```python
import torch

def _softmax(x, axis=-1):
    return torch.softmax(x, dim=axis)
def _gelu(x):
    return torch.nn.functional.gelu(x, approximate="tanh")
def _silu(x):
    return torch.nn.functional.silu(x)
def _sigmoid(x):
    return torch.sigmoid(x)
def _layernorm(x, axis=-1, eps=1e-5):
    m = x.mean(dim=axis, keepdim=True)
    v = x.var(dim=axis, keepdim=True, unbiased=False)
    return (x - m) / torch.sqrt(v + eps)
def _scatter_add(tgt, idx, src, axis=0):
    return tgt.clone().index_add_(axis, idx, src)
def _segment_sum(src, seg, n):
    out = torch.zeros((n,) + src.shape[1:], dtype=src.dtype)
    return out.index_add_(0, seg, src)
def _pad(x, pw):
    flat = tuple(v for pair in reversed(pw) for v in pair)
    return torch.nn.functional.pad(x, flat)

kernel_name = 'recurrent_scan__sliding_block_cached__mixer_token_mlp__decode'

def setup_inputs():

    _w = (lambda a: (lambda t: t.to(torch.bfloat16) if t.is_floating_point() else t)(torch.as_tensor(a)))
    def _A_setup():
        B, Sq, D = 1, 1, 512
        Hq, Hkv, Sk, Dh = 32, 8, 32768, 128
        assert Hq % Hkv == 0, (Hq, Hkv)
        return {
            "x": _w((lambda *s: torch.randn(*s, dtype=torch.bfloat16) * 0.4)(B, Sq, D)),
            "k_cache": _w((lambda *s: torch.randn(*s, dtype=torch.bfloat16) * 1.6)(B, Hkv, Sk, Dh)),
            "v_cache": _w((lambda *s: torch.randn(*s, dtype=torch.bfloat16) * 1.6)(B, Hkv, Sk, Dh)),
            "wq": _w((lambda *s: torch.randn(*s, dtype=torch.bfloat16) * 1.6)(D, Hq * Dh) * (D ** -0.5)),
            "wo": _w((lambda *s: torch.randn(*s, dtype=torch.bfloat16) * 1.6)(Hq * Dh, D) * ((Hq * Dh) ** -0.5)),
        }
    _A_in = _A_setup()

    def _B_setup():
        B, S, D, F = 1, 1, 512, 1792
        return {
            "x": _w((lambda *s: torch.randn(*s, dtype=torch.bfloat16) * 0.4)(B, S, D)),
            "w1": _w((lambda *s: torch.randn(*s, dtype=torch.bfloat16) * 1.6)(S, F) * (S ** -0.5)),
            "b1": _w((lambda *s: torch.randn(*s, dtype=torch.bfloat16) * 1.6)(F) * 0.02),
            "w2": _w((lambda *s: torch.randn(*s, dtype=torch.bfloat16) * 1.6)(F, S) * (F ** -0.5)),
            "b2": _w((lambda *s: torch.randn(*s, dtype=torch.bfloat16) * 1.6)(S) * 0.02),
        }
    _B_in = _B_setup()

    return {
        "A_x": _A_in["x"],
        "A_k_cache": _A_in["k_cache"],
        "A_v_cache": _A_in["v_cache"],
        "A_wq": _A_in["wq"],
        "A_wo": _A_in["wo"],
        "B_x": _B_in["x"],
        "B_w1": _B_in["w1"],
        "B_b1": _B_in["b1"],
        "B_w2": _B_in["w2"],
        "B_b2": _B_in["b2"],
    }

def reference(A_x, A_k_cache, A_v_cache, A_wq, A_wo, B_x, B_w1, B_b1, B_w2, B_b2):
    def _A_ref(x, k_cache, v_cache, wq, wo):
        B, Sq, D = x.shape
        Hq, Dh = 32, 128
        Hkv, Sk = k_cache.shape[1], k_cache.shape[2]
        W = min(4096, max(256, Sk // 4))
        rep = Hq // Hkv
        q = (lambda x, *d: x.permute(*d))((x @ wq).reshape(B, Sq, Hq, Dh), 0, 2, 1, 3)
        k = (lambda x, n, axis: torch.repeat_interleave(x, n, dim=axis))(k_cache[:, :, Sk - W:, :], rep, axis=1)
        v = (lambda x, n, axis: torch.repeat_interleave(x, n, dim=axis))(v_cache[:, :, Sk - W:, :], rep, axis=1)
        scores = torch.einsum("bhqd,bhkd->bhqk", q, k) * (Dh ** -0.5)
        p = _softmax(scores, axis=-1)
        o = torch.einsum("bhqk,bhkd->bhqd", p, v)
        return (lambda x, *d: x.permute(*d))(o, 0, 2, 1, 3).reshape(B, Sq, Hq * Dh) @ wo

    def _B_ref(x, w1, b1, w2, b2):
        B, S, D = x.shape
        h = _layernorm(x, axis=-1)
        u = torch.einsum("bsd,sf->bdf", h, w1) + b1
        u = _gelu(u)
        y = torch.einsum("bdf,fs->bds", u, w2) + b2
        return (lambda x, *d: x.permute(*d))(y, 0, 2, 1)

    x = A_x
    xn = _layernorm(x, axis=-1)
    s = _A_ref(xn, A_k_cache, A_v_cache, A_wq, A_wo)
    g = _silu(_B_ref(xn, B_w1, B_b1, B_w2, B_b2))
    return x + s * g
```

```cpp
#include <hip/hip_runtime.h>
#include <hip/hip_bf16.h>
#include <stdint.h>

typedef unsigned short u16;
typedef unsigned v4u __attribute__((ext_vector_type(4)));

__device__ __forceinline__ float bf2f(u16 b) { return __uint_as_float(((unsigned)b) << 16); }
__device__ __forceinline__ float rbf(float f) {
  return __bfloat162float(__float2bfloat16(f));
}
__device__ __forceinline__ float wsum(float v) {
  for (int o = 32; o >= 1; o >>= 1) v += __shfl_xor(v, o);
  return v;
}
__device__ __forceinline__ float wmax(float v) {
  for (int o = 32; o >= 1; o >>= 1) v = fmaxf(v, __shfl_xor(v, o));
  return v;
}
__device__ __forceinline__ void ln8(const float* x, float* y) {
  float s = 0; for (int j = 0; j < 8; ++j) s += x[j];
  s = wsum(s); float mean = s * (1.0f / 512.0f);
  float q = 0; for (int j = 0; j < 8; ++j) { float d = x[j] - mean; q += d * d; }
  q = wsum(q); float var = rbf(q * (1.0f / 512.0f));
  float m = rbf(mean);
  float sd = rbf(sqrtf(rbf(var + 1e-5f)));
  for (int j = 0; j < 8; ++j) y[j] = rbf(rbf(x[j] - m) / sd);
}

__global__ void k1(const u16* x, const u16* wq, const u16* w1, const u16* b1, const u16* w2, const u16* b2, float* q, float* g) {
  __shared__ float xn[512], hh[512], red[256 * 2];
  int tid = threadIdx.x, lane = tid & 63;
  if (tid < 64) {
    float xv[8], y[8], h[8];
    for (int j = 0; j < 8; ++j) xv[j] = bf2f(x[lane * 8 + j]);
    ln8(xv, y); ln8(y, h);
    for (int j = 0; j < 8; ++j) { xn[lane * 8 + j] = y[j]; hh[lane * 8 + j] = h[j]; }
  }
  __syncthreads();
  int c = blockIdx.x * 16 + (tid & 15), r0 = tid >> 4;
  float acc = 0;
  for (int r = r0; r < 512; r += 16) acc += xn[r] * bf2f(wq[(size_t)r * 4096 + c]);
  red[tid] = acc; __syncthreads();
  if (tid < 16) { float s = 0; for (int i = 0; i < 16; ++i) s += red[i * 16 + tid]; q[blockIdx.x * 16 + tid] = rbf(s); }
  __syncthreads();
  float a0 = 0, a1 = 0; float h0 = hh[blockIdx.x * 2], h1 = hh[blockIdx.x * 2 + 1];
  for (int f = tid; f < 1792; f += 256) {
    float w = bf2f(w1[f]), b = bf2f(b1[f]), v = bf2f(w2[f]);
    float u0 = rbf(rbf(h0 * w) + b), u1 = rbf(rbf(h1 * w) + b);
    float t0 = tanhf(0.7978845608028654f * (u0 + 0.044715f * u0 * u0 * u0));
    float t1 = tanhf(0.7978845608028654f * (u1 + 0.044715f * u1 * u1 * u1));
    a0 += rbf(0.5f * u0 * (1.f + t0)) * v; a1 += rbf(0.5f * u1 * (1.f + t1)) * v;
  }
  red[tid] = a0; red[256 + tid] = a1; __syncthreads();
  if (tid < 2) { float s = 0; for (int i = 0; i < 256; ++i) s += red[tid * 256 + i];
    float y = rbf(rbf(s) + bf2f(b2[0])); g[blockIdx.x * 2 + tid] = rbf(y / (1.f + __expf(-y))); }
}

__global__ void k2(const u16* K, const u16* V, const float* q, float* po, float* pml) {
  __shared__ float qs[512], sc[512], ml[8];
  int tid = threadIdx.x, kvh = blockIdx.x >> 5, sp = blockIdx.x & 31;
  qs[tid] = q[kvh * 512 + tid]; qs[256 + tid] = q[kvh * 512 + 256 + tid];
  __syncthreads();
  size_t base = ((size_t)kvh * 32768 + (32768 - 4096) + sp * 128) * 128;
  for (int i = tid; i < 512; i += 256) { int h = i >> 7, key = i & 127; float a = 0;
    for (int d = 0; d < 128; ++d) a += qs[h * 128 + d] * bf2f(K[base + key * 128 + d]);
    sc[i] = rbf(rbf(a) * 0.08838834764831845f); }
  __syncthreads();
  int w = tid >> 6, lane = tid & 63;
  { float a = sc[w * 128 + lane], b = sc[w * 128 + 64 + lane]; float m = wmax(fmaxf(a, b));
    a = __expf(a - m); b = __expf(b - m); float l = wsum(a + b); sc[w * 128 + lane] = a; sc[w * 128 + 64 + lane] = b;
    if (lane == 0) { pml[(blockIdx.x * 4 + w) * 2] = m; pml[(blockIdx.x * 4 + w) * 2 + 1] = l; } }
  __syncthreads();
  for (int i = tid; i < 512; i += 256) { int h = i >> 7, d = i & 127; float a = 0;
    for (int key = 0; key < 128; ++key) a += sc[h * 128 + key] * bf2f(V[base + key * 128 + d]);
    po[blockIdx.x * 512 + i] = a; }
}

__global__ void k3(const u16* wo, const float* po, const float* pml, float* ps) {
  __shared__ float o[16];
  int tid = threadIdx.x, kvh = blockIdx.x >> 5, j = blockIdx.x & 31, h = j >> 3;
  if (tid < 16) { float M = -1e30f; for (int p = 0; p < 32; ++p) M = fmaxf(M, pml[((kvh * 32 + p) * 4 + h) * 2]);
    float L = 0, a = 0; for (int p = 0; p < 32; ++p) { float w = __expf(pml[((kvh * 32 + p) * 4 + h) * 2] - M); L += w * pml[((kvh * 32 + p) * 4 + h) * 2 + 1]; a += w * po[(kvh * 32 + p) * 512 + j * 16 + tid]; }
    o[tid] = rbf(a / L); }
  __syncthreads();
  for (int c = tid; c < 512; c += 256) { float a = 0; for (int r = 0; r < 16; ++r) a += o[r] * bf2f(wo[(size_t)(blockIdx.x * 16 + r) * 512 + c]); ps[blockIdx.x * 512 + c] = a; }
}
__global__ void k4(const u16* x, const float* ps, const float* g, __hip_bfloat16* out) {
  int c = blockIdx.x * 64 + threadIdx.x; float a = 0; for (int p = 0; p < 256; ++p) a += ps[p * 512 + c];
  out[c] = __float2bfloat16(bf2f(x[c]) + rbf(rbf(a) * g[c]));
}

extern "C" void kernel_launch(void* const* d_in, const int* in_sizes, int n_in, void* d_out, int out_size, void* d_ws, size_t ws_size, hipStream_t stream) {
  const u16* x = (const u16*)d_in[0]; const u16* K = (const u16*)d_in[1]; const u16* V = (const u16*)d_in[2];
  const u16* wq = (const u16*)d_in[3]; const u16* wo = (const u16*)d_in[4];
  const u16* w1 = (const u16*)d_in[6]; const u16* b1 = (const u16*)d_in[7]; const u16* w2 = (const u16*)d_in[8]; const u16* b2 = (const u16*)d_in[9];
  float* ws = (float*)d_ws; float* q = ws; float* g = ws + 4096; float* pml = ws + 8192; float* po = ws + 16384; float* ps = po + 256 * 512;
  k1<<<256, 256, 0, stream>>>(x, wq, w1, b1, w2, b2, q, g);
  k2<<<256, 256, 0, stream>>>(K, V, q, po, pml);
  k3<<<256, 256, 0, stream>>>(wo, po, pml, ps);
  k4<<<8, 64, 0, stream>>>(x, ps, g, (__hip_bfloat16*)d_out);
}
```

```cpp
#include <hip/hip_runtime.h>
#include <hip/hip_bf16.h>
#include <stdint.h>

#define STAMPS 0
#define KV_INIT 8
#define KV_WIN 8
#define POLL_SLEEP 0
#define BACKOFF() do { if (POLL_SLEEP) __builtin_amdgcn_s_sleep(POLL_SLEEP); } while (0)
#define KV_DELAY 0
#define WO_MODE 2

namespace fused {
typedef unsigned short u16;
typedef unsigned long long u64;
typedef unsigned v4u __attribute__((ext_vector_type(4)));
typedef unsigned v2u __attribute__((ext_vector_type(2)));
typedef float f32x4 __attribute__((ext_vector_type(4)));
typedef __bf16 bf16x8 __attribute__((ext_vector_type(8)));
#define GAS __attribute__((address_space(1)))
#define LAS __attribute__((address_space(3)))
#define RLX_AGENT __ATOMIC_RELAXED, __HIP_MEMORY_SCOPE_AGENT
#define RLX_WG __ATOMIC_RELAXED, __HIP_MEMORY_SCOPE_WORKGROUP

constexpr int WS_EPOCH = 0;
constexpr int WS_Q = 4096;
constexpr int WS_ML = WS_Q + 131072;
constexpr int WS_O = WS_ML + 32768;
constexpr int WS_S = WS_O + 524288;
constexpr int WS_END = WS_S + 131072;
constexpr int WS_STAMP = 1 << 20;
constexpr unsigned SENT = 0xFFFFFFFFu;
constexpr int SPIN_CAP = 1 << 16;
constexpr int AUX_SC1 = 16;

constexpr int L_CNT = 0;
constexpr int L_GATE = 64;
constexpr int L_ML = 128;
constexpr int L_OIMG = 256;
constexpr int L_QPART = 512;
constexpr int L_QIMG = 1536;
constexpr int L_OW = 2560;
constexpr int OW_H = 528, OW_W = 4 * OW_H;
constexpr int L_WO = L_OW + 4 * OW_W;
constexpr int L_X = L_WO + 16384;
constexpr int L_END = L_X + 4096;
static_assert(L_WO % 16 == 0, "align");

__device__ __forceinline__ float u2f(unsigned u) { return __builtin_bit_cast(float, u); }
__device__ __forceinline__ unsigned f2u(float f) { return __builtin_bit_cast(unsigned, f); }
__device__ __forceinline__ float bflo(unsigned u) { return u2f(u << 16); }
__device__ __forceinline__ float bfhi(unsigned u) { return u2f(u & 0xffff0000u); }
typedef __bf16 bf16x2 __attribute__((ext_vector_type(2)));
typedef float f32x2 __attribute__((ext_vector_type(2)));
__device__ __forceinline__ unsigned pkbf(float lo, float hi) {
  f32x2 v = {lo, hi};
  return __builtin_bit_cast(unsigned, __builtin_convertvector(v, bf16x2));
}
__device__ __forceinline__ float rbf(float f) { return bflo(pkbf(f, f)); }
__device__ __forceinline__ void rbf2(float& a, float& b) { unsigned p = pkbf(a, b); a = bflo(p); b = bfhi(p); }
template <int CTRL> __device__ __forceinline__ float dpp(float x) { return u2f(__builtin_amdgcn_mov_dpp(f2u(x), CTRL, 0xf, 0xf, true)); }
constexpr int SHL4 = 0x104, SHL8 = 0x108, SHL12 = 0x10C, ROR1 = 0x121, ROR2 = 0x122, ROR4 = 0x124, ROR8 = 0x128;
__device__ __forceinline__ float sw32add(float a, float b) { auto r = __builtin_amdgcn_permlane32_swap(f2u(a), f2u(b), false, false); return u2f(r[0]) + u2f(r[1]); }
__device__ __forceinline__ float sw16add(float a, float b) { auto r = __builtin_amdgcn_permlane16_swap(f2u(a), f2u(b), false, false); return u2f(r[0]) + u2f(r[1]); }
__device__ __forceinline__ float sw32max(float a) { auto r = __builtin_amdgcn_permlane32_swap(f2u(a), f2u(a), false, false); return fmaxf(u2f(r[0]), u2f(r[1])); }
__device__ __forceinline__ float sw16max(float a) { auto r = __builtin_amdgcn_permlane16_swap(f2u(a), f2u(a), false, false); return fmaxf(u2f(r[0]), u2f(r[1])); }
__device__ __forceinline__ float rowsum(float v) { v += dpp<ROR8>(v); v += dpp<ROR4>(v); v += dpp<ROR2>(v); v += dpp<ROR1>(v); return v; }
__device__ __forceinline__ float wavesum(float v) { v = rowsum(v); v = sw16add(v, v); return sw32add(v, v); }
__device__ __forceinline__ f32x4 mfma(v4u a, v4u b, f32x4 c) { return __builtin_amdgcn_mfma_f32_16x16x32_bf16(__builtin_bit_cast(bf16x8, a), __builtin_bit_cast(bf16x8, b), c, 0, 0, 0); }
__device__ __forceinline__ unsigned nosent(float f) { return min(f2u(f), 0xFFFFFFFEu); }

__device__ __forceinline__ void ld16nt(v4u& d, const void* base, unsigned off) { asm volatile("global_load_dwordx4 %0, %1, %2 nt" : "=v"(d) : "v"(off), "s"(base) : "memory"); }
__device__ __forceinline__ void ld16(v4u& d, const void* base, unsigned off) { asm volatile("global_load_dwordx4 %0, %1, %2" : "=v"(d) : "v"(off), "s"(base) : "memory"); }
__device__ __forceinline__ void ld4(unsigned& d, const void* base, unsigned off) { asm volatile("global_load_dword %0, %1, %2" : "=v"(d) : "v"(off), "s"(base) : "memory"); }
__device__ __forceinline__ void ld2(unsigned& d, const void* base, unsigned off) { asm volatile("global_load_ushort %0, %1, %2" : "=v"(d) : "v"(off), "s"(base) : "memory"); }
__device__ __forceinline__ void ld8sc1(v2u& d, const void* base, unsigned off) { asm volatile("global_load_dwordx2 %0, %1, %2 sc1" : "=v"(d) : "v"(off), "s"(base) : "memory"); }
__device__ __forceinline__ void dma16nt(const void* base, unsigned off, unsigned lds) {
  unsigned keep;
  asm volatile("s_mov_b32 %0, m0\n\ts_mov_b32 m0, %3\n\ts_nop 0\n\tglobal_load_lds_dwordx4 %1, %2 nt\n\ts_mov_b32 m0, %0" : "=&s"(keep) : "v"(off), "s"(base), "s"(lds) : "memory");
}
__device__ __forceinline__ void poll4x16(v4u (&a)[4], const void* base, unsigned off) {
  asm volatile("global_load_dwordx4 %0, %4, %5 sc1\n\tglobal_load_dwordx4 %1, %4, %6 sc1\n\tglobal_load_dwordx4 %2, %4, %7 sc1\n\tglobal_load_dwordx4 %3, %4, %8 sc1\n\ts_waitcnt vmcnt(0)"
               : "=&v"(a[0]), "=&v"(a[1]), "=&v"(a[2]), "=&v"(a[3]) : "v"(off), "s"(base), "s"((const char*)base + 4096), "s"((const char*)base + 8192), "s"((const char*)base + 12288) : "memory");
}
__device__ __forceinline__ void poll2x16(v4u& a, v4u& c, const void* base0, const void* base1, unsigned off) {
  asm volatile("global_load_dwordx4 %0, %2, %3 sc1\n\tglobal_load_dwordx4 %1, %2, %4 sc1\n\ts_waitcnt vmcnt(0)" : "=&v"(a), "=&v"(c) : "v"(off), "s"(base0), "s"(base1) : "memory");
}
__device__ __forceinline__ unsigned lds_inc_rtn(unsigned addr) {
  unsigned r, one = 1; u64 keep;
  asm volatile("s_mov_b64 %1, exec\n\ts_mov_b64 exec, 1\n\tds_add_rtn_u32 %0, %2, %3\n\ts_mov_b64 exec, %1\n\ts_waitcnt lgkmcnt(0)" : "=&v"(r), "=&s"(keep) : "v"(addr), "v"(one) : "memory");
  return __builtin_amdgcn_readfirstlane(r);
}
__device__ __forceinline__ void lds_inc(unsigned addr) {
  unsigned one = 1; u64 keep;
  asm volatile("s_mov_b64 %0, exec\n\ts_mov_b64 exec, 1\n\tds_add_u32 %1, %2\n\ts_mov_b64 exec, %0" : "=&s"(keep) : "v"(addr), "v"(one) : "memory");
}
#define WAITVM(N) asm volatile("s_waitcnt vmcnt(%0)" ::"n"(N) : "memory")
#define PIN(x) asm volatile("" : "+v"(x))
#define BARRIER() asm volatile("s_waitcnt lgkmcnt(0)\n\ts_barrier" ::: "memory")
#define STAMP(k) do {} while (0)

struct LN { float m, sd, rs; };
__device__ __forceinline__ LN ln_stats(const float (&x)[8]) {
  float s = ((x[0] + x[1]) + (x[2] + x[3])) + ((x[4] + x[5]) + (x[6] + x[7]));
  const float mean = wavesum(s) * (1.0f / 512.0f);
  float q = 0.f;
#pragma unroll
  for (int i = 0; i < 8; ++i) { const float d = x[i] - mean; q = fmaf(d, d, q); }
  const float var = rbf(wavesum(q) * (1.0f / 512.0f));
  LN r; r.m = rbf(mean); r.sd = rbf(sqrtf(rbf(var + 1e-5f))); r.rs = __builtin_amdgcn_rcpf(r.sd);
  return r;
}
__device__ __forceinline__ float ln_apply(float x, LN s) {
  const float d = rbf(x - s.m), q = d * s.rs;
  return rbf(fmaf(fmaf(-q, s.sd, d), s.rs, q));
}

template <int I> struct KVIssue {
  __device__ static __forceinline__ void run(v4u (&kv)[16], const char* Kb, const char* Vb, unsigned koff, unsigned voff) {
    if constexpr (I < 16) {
      if constexpr (I == KV_INIT && KV_INIT < KV_WIN) WAITVM(KV_INIT - 1);
      if constexpr (I >= KV_WIN) WAITVM(KV_WIN - 1);
      if constexpr (I < 8) ld16nt(kv[I], Kb + I * 4096, koff); else ld16nt(kv[I], Vb + (I - 8) * 4096, voff);
      KVIssue<I + 1>::run(kv, Kb, Vb, koff, voff);
    }
  }
};

__global__ void __launch_bounds__(768, 3)
fused_kernel(const char* __restrict__ x, const char* __restrict__ K, const char* __restrict__ V, const char* __restrict__ wq, const char* __restrict__ wo,
             const char* __restrict__ w1, const char* __restrict__ b1, const char* __restrict__ w2, const char* __restrict__ b2,
             u16* __restrict__ out, char* __restrict__ ws) {
  __shared__ __attribute__((aligned(16))) char smem[L_END];
  LAS char* const L = (LAS char*)smem;
  STAMP(0);
  const int b = blockIdx.x, g = b & 7, j = b >> 3;
  const int lane = threadIdx.x & 63, w8 = __builtin_amdgcn_readfirstlane(threadIdx.x >> 6), w = w8 & 3;
  const int rb = j >> 2, cb = j & 3, hc = rb >> 1, hb = rb & 1;
  const int c0 = 64 * g + 2 * j;
  const __amdgpu_buffer_rsrc_t rs = __builtin_amdgcn_make_buffer_rsrc(ws, 0, WS_END, 0x00020000);
  LAS unsigned* cnt = (LAS unsigned*)(L + L_CNT);

  if (w8 < 4) {
    __builtin_amdgcn_s_setprio(1);
    const int kq = j >> 3, jc = j & 7;
    const int r0 = 128 * kq + 8 * w + (lane >> 3);
    v2u ep; v4u wv[4], xv;
    ld16(xv, x, lane * 16);
    const unsigned qoff = (unsigned)r0 * 8192u + (g * 512 + jc * 64 + (lane & 7) * 8) * 2;
#pragma unroll
    for (int i = 0; i < 4; ++i) ld16nt(wv[i], wq + (size_t)i * 32 * 8192, qoff);
    ld8sc1(ep, ws, WS_EPOCH);
    {
      int off = WS_O + (g * 32 + j) * 2048 + w * 512 + lane * 16;
      if (lane >= 32) off = w == 0 ? WS_S + (cb * 64 + g * 8 + rb) * 512 + (lane - 32) * 16 : (w == 1 && lane < 34) ? WS_ML + (g * 32 + j) * 128 + (lane - 32) * 16 : WS_END;
      const v4u s4 = {SENT, SENT, SENT, SENT};
      asm volatile("buffer_store_dwordx4 %0, %1, %2, 0 offen sc1\n\ts_nop 1" :: "v"(s4), "v"(off), "s"(rs) : "memory");
    }
    if (threadIdx.x < 4) cnt[threadIdx.x] = 0;
    BARRIER();
    WAITVM(6); PIN(xv);
    STAMP(1);
    *(LAS v4u*)(L + L_X + w * 1024 + lane * 16) = xv;
    unsigned xr[4];
#pragma unroll
    for (int i = 0; i < 4; ++i) xr[i] = *(const LAS u16*)(L + L_X + w * 1024 + r0 * 2 + i * 64);
    const unsigned xp = *(const LAS unsigned*)(L + L_X + w * 1024 + c0 * 2);
    float xf[8] = {bflo(xv.x), bfhi(xv.x), bflo(xv.y), bfhi(xv.y), bflo(xv.z), bfhi(xv.z), bflo(xv.w), bfhi(xv.w)};
    const LN s1 = ln_stats(xf);
    float xn[4];
#pragma unroll
    for (int i = 0; i < 4; ++i) { xn[i] = ln_apply(bflo(xr[i]), s1); PIN(xn[i]); }
    float acc[8];
#pragma unroll
    for (int e = 0; e < 8; ++e) acc[e] = 0.f;
#pragma unroll
    for (int i = 0; i < 4; ++i) {
      if (i == 0) { WAITVM(5); } else if (i == 1) { WAITVM(4); } else if (i == 2) { WAITVM(3); } else { WAITVM(2); }
      PIN(wv[i]);
#pragma unroll
      for (int d = 0; d < 4; ++d) { acc[2 * d] = fmaf(xn[i], bflo(wv[i][d]), acc[2 * d]); acc[2 * d + 1] = fmaf(xn[i], bfhi(wv[i][d]), acc[2 * d + 1]); }
#pragma unroll
      for (int e = 0; e < 8; ++e) PIN(acc[e]);
    }
    STAMP(2);
    {
      float y[4], z[2];
#pragma unroll
      for (int e = 0; e < 4; ++e) y[e] = sw32add(acc[e], acc[e + 4]);
#pragma unroll
      for (int e = 0; e < 2; ++e) z[e] = sw16add(y[e], y[e + 2]);
      const float t0 = z[0] + dpp<ROR8>(z[0]), t1 = z[1] + dpp<ROR8>(z[1]);
      ((LAS float*)(L + L_QPART))[w * 64 + (lane & 7) * 8 + (lane >> 3)] = (lane & 8) ? t1 : t0;
    }
    WAITVM(0); PIN(ep);
    const unsigned e_lo = __builtin_amdgcn_readfirstlane(ep.x), e_hi = __builtin_amdgcn_readfirstlane(ep.y);
    const u64 epoch1 = (((u64)e_hi << 32) | e_lo) + 1;
    const unsigned t8 = (unsigned)epoch1 & 0xFFu, t32 = (unsigned)(epoch1 >> 8);
    const unsigned old = lds_inc_rtn((unsigned)(size_t)(L + L_CNT));
    if (old == 3) {
      const LAS float* qp = (const LAS float*)(L + L_QPART) + lane;
      const float v = (qp[0] + qp[64]) + (qp[128] + qp[192]);
      const u64 gr = ((u64)t32 << 32) | ((f2u(v) & 0xFFFFFF00u) | t8);
      __hip_atomic_store((GAS u64*)(ws + WS_Q + g * 16384 + kq * 4096 + jc * 512) + lane, gr, RLX_AGENT);
    }
    STAMP(3);
    {
      v4u a[4];
      for (int spin = 0; spin < SPIN_CAP; ++spin) {
        unsigned bad = 0;
        poll4x16(a, ws + WS_Q + g * 16384 + w * 1024, lane * 16);
#pragma unroll
        for (int k = 0; k < 4; ++k) bad |= (((a[k].x ^ t8) | (a[k].z ^ t8)) & 0xFFu) | (a[k].y ^ t32) | (a[k].w ^ t32);
        if (__builtin_amdgcn_ballot_w64(bad != 0) == 0) break;
        BACKOFF();
      }
      const float q0 = (u2f(a[0].x & 0xFFFFFF00u) + u2f(a[1].x & 0xFFFFFF00u)) + (u2f(a[2].x & 0xFFFFFF00u) + u2f(a[3].x & 0xFFFFFF00u));
      const float q1 = (u2f(a[0].z & 0xFFFFFF00u) + u2f(a[1].z & 0xFFFFFF00u)) + (u2f(a[2].z & 0xFFFFFF00u) + u2f(a[3].z & 0xFFFFFF00u));
      ((LAS unsigned*)(L + L_QIMG))[w * 64 + lane] = pkbf(q0, q1);
    }
    STAMP(4);
    BARRIER();
    float gate = 0.f;
    if (w == 0) {
      const LAS float* gp = (const LAS float*)(L + L_GATE) + (lane >> 5);
      const float y = rbf(rbf((gp[0] + gp[2]) + (gp[4] + gp[6])) + bflo(*(const GAS u16*)b2));
      gate = rbf(y / (1.0f + __expf(-y)));
      const int jp = 4 * (lane & 7) + (g >> 1);
      const GAS u64* tp = (const GAS u64*)(ws + WS_Q + (lane >> 3) * 16384 + (jp >> 3) * 4096 + (jp & 7) * 512);
      for (int spin = 0; spin < SPIN_CAP; ++spin) {
        const u64 t = __hip_atomic_load(tp, RLX_AGENT);
        if (__builtin_amdgcn_ballot_w64((((unsigned)t ^ t8) & 0xFFu) != 0 || (unsigned)(t >> 32) != t32) == 0) break;
      }
    }
    while (__hip_atomic_load(cnt + 1, RLX_WG) < 4u) __builtin_amdgcn_s_sleep(1);
    asm volatile("" ::: "memory");
    const int hs = w == 0 ? (lane & 3) : w;
    const f32x4 m4 = *(const LAS f32x4*)(L + L_ML + hs * 16), l4 = *(const LAS f32x4*)(L + L_ML + 64 + hs * 16);
    const float mcu = fmaxf(fmaxf(m4.x, m4.y), fmaxf(m4.z, m4.w));
    float f0 = __expf(m4.x - mcu), f1 = __expf(m4.y - mcu), f2 = __expf(m4.z - mcu), f3 = __expf(m4.w - mcu);
    if (w == 0 && lane < 4) {
      const float lcu = fmaf(f0, l4.x, f1 * l4.y) + fmaf(f2, l4.z, f3 * l4.w);
      __hip_atomic_store((GAS u64*)(ws + WS_ML + (g * 32 + j) * 128) + lane, ((u64)nosent(lcu) << 32) | nosent(mcu), RLX_AGENT);
    }
    f0 = u2f(__builtin_amdgcn_readfirstlane(f2u(f0))); f1 = u2f(__builtin_amdgcn_readfirstlane(f2u(f1)));
    f2 = u2f(__builtin_amdgcn_readfirstlane(f2u(f2))); f3 = u2f(__builtin_amdgcn_readfirstlane(f2u(f3)));
    STAMP(5);
    BARRIER();
    STAMP(6);
    if (lane < 32) {
      const LAS char* p = L + L_OW + w * OW_H + lane * 16;
      const f32x4 o0 = *(const LAS f32x4*)p, o1 = *(const LAS f32x4*)(p + OW_W), o2 = *(const LAS f32x4*)(p + 2 * OW_W), o3 = *(const LAS f32x4*)(p + 3 * OW_W);
      const f32x4 o = (o0 * f0 + o1 * f1) + (o2 * f2 + o3 * f3);
      const v4u ou = {nosent(o.x), nosent(o.y), nosent(o.z), nosent(o.w)};
      __builtin_amdgcn_raw_buffer_store_b128(ou, rs, WS_O + (g * 32 + j) * 2048 + w * 512 + lane * 16, 0, AUX_SC1);
    }
    STAMP(7);
    float wa, wb;
    {
      const GAS u64* mp = (const GAS u64*)(ws + WS_ML + (g * 32 + (lane >> 2)) * 128 + hc * 8);
      u64 a, c;
      for (int spin = 0; spin < SPIN_CAP; ++spin) {
        a = __hip_atomic_load(mp, RLX_AGENT); c = __hip_atomic_load(mp + 256, RLX_AGENT);
        if (__builtin_amdgcn_ballot_w64((unsigned)a == SENT || (unsigned)(a >> 32) == SENT || (unsigned)c == SENT || (unsigned)(c >> 32) == SENT) == 0) break;
        BACKOFF();
      }
      const float ma = u2f((unsigned)a), mb = u2f((unsigned)c);
      float M = fmaxf(ma, mb); M = fmaxf(M, dpp<ROR4>(M)); M = fmaxf(M, dpp<ROR8>(M)); M = sw32max(sw16max(M));
      wa = __expf(ma - M); wb = __expf(mb - M);
      float Ls = fmaf(wa, u2f((unsigned)(a >> 32)), wb * u2f((unsigned)(c >> 32)));
      Ls += dpp<ROR4>(Ls); Ls += dpp<ROR8>(Ls); Ls = sw16add(Ls, Ls); Ls = sw32add(Ls, Ls);
      const float inv = __builtin_amdgcn_rcpf(Ls); wa *= inv; wb *= inv;
    }
    STAMP(8);
    {
      const char* ob = ws + WS_O + g * 65536 + hc * 512 + hb * 256 + w * 64;
      const unsigned ooff = (lane >> 2) * 2048 + (lane & 3) * 16;
      v4u a, c;
      for (int spin = 0; spin < SPIN_CAP; ++spin) {
        poll2x16(a, c, ob, ob + 16 * 2048, ooff);
        const unsigned mx = max(max(max(a.x, a.y), max(a.z, a.w)), max(max(c.x, c.y), max(c.z, c.w)));
        if (__builtin_amdgcn_ballot_w64(mx == SENT) == 0) break;
        BACKOFF();
      }
      STAMP(9);
      float v[4];
#pragma unroll
      for (int i = 0; i < 4; ++i) v[i] = fmaf(wa, u2f(a[i]), wb * u2f(c[i]));
      float t = sw16add(sw32add(v[0], v[2]), sw32add(v[1], v[3]));
      t += dpp<ROR4>(t); t += dpp<ROR8>(t);
      const int cpos = 4 * (lane & 3) + 2 * (lane >> 5) + ((lane >> 4) & 1);
      ((LAS u16*)(L + L_OIMG))[4 * cpos + w] = (u16)pkbf(t, t);
    }
    BARRIER();
    if (w == 0) {
      const GAS u64* sp = (const GAS u64*)(ws + WS_S + ((g >> 1) * 64 + lane) * 512 + ((g & 1) * 64 + 2 * j) * 4);
      u64 a;
      for (int spin = 0; spin < SPIN_CAP; ++spin) {
        a = __hip_atomic_load(sp, RLX_AGENT);
        if (__builtin_amdgcn_ballot_w64((unsigned)a == SENT || (unsigned)(a >> 32) == SENT) == 0) break;
        BACKOFF();
      }
      STAMP(10);
      float t = sw32add(u2f((unsigned)a), u2f((unsigned)(a >> 32)));
      t = rowsum(t); t = sw16add(t, t);
      const float xo = (lane & 32) ? bfhi(xp) : bflo(xp);
      const float r = xo + rbf(rbf(t) * gate);
      if ((lane & 31) == 0) out[c0 + (lane >> 5)] = (u16)pkbf(r, r);
      if (b == 0 && lane == 0) __hip_atomic_store((GAS u64*)(ws + WS_EPOCH), epoch1, RLX_AGENT);
      STAMP(11);
    }
  } else if (w8 >= 8) {
    BARRIER();
    v4u g1, gb, g2, xv; unsigned xp;
    ld16(xv, x, lane * 16);
    ld4(xp, x, c0 * 2);
    {
      const int fo = (w * 64 + lane) * 16;
      const __amdgpu_buffer_rsrc_t r1 = __builtin_amdgcn_make_buffer_rsrc((void*)w1, 0, 3584, 0x00020000), rb1 = __builtin_amdgcn_make_buffer_rsrc((void*)b1, 0, 3584, 0x00020000),
                                   r2 = __builtin_amdgcn_make_buffer_rsrc((void*)w2, 0, 3584, 0x00020000);
      asm volatile("buffer_load_dwordx4 %0, %1, %2, 0 offen" : "=v"(g1) : "v"(fo), "s"(r1) : "memory");
      asm volatile("buffer_load_dwordx4 %0, %1, %2, 0 offen" : "=v"(gb) : "v"(fo), "s"(rb1) : "memory");
      asm volatile("buffer_load_dwordx4 %0, %1, %2, 0 offen" : "=v"(g2) : "v"(fo), "s"(r2) : "memory");
    }
    WAITVM(0); PIN(xv); PIN(xp); PIN(g1); PIN(gb); PIN(g2);
    STAMP(1);
    {
      float xf[8] = {bflo(xv.x), bfhi(xv.x), bflo(xv.y), bfhi(xv.y), bflo(xv.z), bfhi(xv.z), bflo(xv.w), bfhi(xv.w)};
      const LN s1 = ln_stats(xf);
#pragma unroll
      for (int i = 0; i < 8; ++i) xf[i] = ln_apply(xf[i], s1);
      const LN s2 = ln_stats(xf);
      const float h0 = ln_apply(ln_apply(bflo(xp), s1), s2), h1 = ln_apply(ln_apply(bfhi(xp), s1), s2);
      constexpr float K0 = -2.0f * 0.7978845608028654f * 1.4426950408889634f, K1 = K0 * 0.044715f;
      float a0 = 0.f, a1 = 0.f;
#pragma unroll
      for (int e = 0; e < 8; ++e) {
        const float wv_ = (e & 1) ? bfhi(g1[e >> 1]) : bflo(g1[e >> 1]), bv = (e & 1) ? bfhi(gb[e >> 1]) : bflo(gb[e >> 1]), vv = (e & 1) ? bfhi(g2[e >> 1]) : bflo(g2[e >> 1]);
        float u0 = h0 * wv_, u1 = h1 * wv_; rbf2(u0, u1);
        u0 += bv; u1 += bv; rbf2(u0, u1);
        float y0 = u0 * __builtin_amdgcn_rcpf(1.0f + __builtin_amdgcn_exp2f(u0 * fmaf(u0 * u0, K1, K0)));
        float y1 = u1 * __builtin_amdgcn_rcpf(1.0f + __builtin_amdgcn_exp2f(u1 * fmaf(u1 * u1, K1, K0)));
        rbf2(y0, y1);
        a0 = fmaf(y0, vv, a0); a1 = fmaf(y1, vv, a1);
      }
      float t = sw32add(a0, a1); t = rowsum(t); t = sw16add(t, t);
      if ((lane & 31) == 0) ((LAS float*)(L + L_GATE))[w * 2 + (lane >> 5)] = t;
    }
    STAMP(2);
  } else {
    __builtin_amdgcn_s_setprio(2);
    v4u kv[16];
    const size_t kb = ((size_t)g * 32768 + 28672 + j * 128) * 256;
    const char* Kb = K + kb; const char* Vb = V + kb;
    const int r = lane & 15, gg = lane >> 4;
    const unsigned koff = w * 1024 + (r >> 2) * 256 + gg * 64 + (r & 3) * 16, voff = w * 1024 + lane * 16;
    BARRIER();
#pragma unroll
    for (int i = 0; i < KV_INIT; ++i) ld16nt(kv[i], Kb + i * 4096, koff);
    STAMP(1);
    KVIssue<KV_INIT>::run(kv, Kb, Vb, koff, voff);
    const unsigned wooff = (unsigned)(g * 512 + hc * 128 + 8 * w + 4 * hb + gg) * 1024u + cb * 256 + r * 16;
    const unsigned wolds = (unsigned)(size_t)(L + L_WO) + w * 1024;
#define ISSUE_WO() _Pragma("unroll") for (int i = 0; i < 4; ++i) dma16nt(wo + i * 32 * 1024, wooff, wolds + i * 4096)
    if (WO_MODE == 0) { ISSUE_WO(); }
    constexpr int XW = WO_MODE == 0 ? 4 : 0;
    STAMP(3);
    BARRIER();
    const v4u qf = *(const LAS v4u*)(L + L_QIMG + (r & 3) * 256 + gg * 64 + (r >> 2) * 16);
    WAITVM(8 + XW);
#pragma unroll
    for (int i = 0; i < 8; ++i) PIN(kv[i]);
    STAMP(4);
    float z[8];
#pragma unroll
    for (int i = 0; i < 8; ++i) {
      const f32x4 X = mfma(kv[i], qf, f32x4{0.f, 0.f, 0.f, 0.f});
      z[i] = (X.x + dpp<SHL4>(X.y)) + (dpp<SHL8>(X.z) + dpp<SHL12>(X.w));
    }
#pragma unroll
    for (int i = 0; i < 8; i += 2) { rbf2(z[i], z[i + 1]); z[i] *= 0.08838834764831845f; z[i + 1] *= 0.08838834764831845f; rbf2(z[i], z[i + 1]); }
    float m = fmaxf(fmaxf(fmaxf(z[0], z[1]), fmaxf(z[2], z[3])), fmaxf(fmaxf(z[4], z[5]), fmaxf(z[6], z[7])));
    m = sw32max(sw16max(m));
    constexpr float LOG2E = 1.4426950408889634f;
    float ls = 0.f;
#pragma unroll
    for (int i = 0; i < 8; ++i) { z[i] = __builtin_amdgcn_exp2f((z[i] - m) * LOG2E); ls += z[i]; }
    ls = sw16add(ls, ls); ls = sw32add(ls, ls);
    const v4u pf = {pkbf(z[0], z[1]), pkbf(z[2], z[3]), pkbf(z[4], z[5]), pkbf(z[6], z[7])};
    if (lane < 4) { ((LAS float*)(L + L_ML))[lane * 4 + w] = m; ((LAS float*)(L + L_ML + 64))[lane * 4 + w] = ls; }
    lds_inc((unsigned)(size_t)(L + L_CNT + 4));
    STAMP(5);
    v4u af[8];
#pragma unroll
    for (int jp = 0; jp < 4; ++jp) {
      if (jp == 0) { WAITVM(6 + XW); } else if (jp == 1) { WAITVM(4 + XW); if (WO_MODE == 1) { ISSUE_WO(); } }
      else if (jp == 2) { WAITVM(2 + (WO_MODE <= 1 ? 4 : 0)); } else { WAITVM(0 + (WO_MODE <= 1 ? 4 : 0)); STAMP(6); if (WO_MODE == 2) { ISSUE_WO(); } }
      PIN(kv[8 + 2 * jp]); PIN(kv[9 + 2 * jp]);
#pragma unroll
      for (int d = 0; d < 4; ++d) {
        af[2 * d][jp] = __builtin_amdgcn_perm(kv[9 + 2 * jp][d], kv[8 + 2 * jp][d], 0x05040100u);
        af[2 * d + 1][jp] = __builtin_amdgcn_perm(kv[9 + 2 * jp][d], kv[8 + 2 * jp][d], 0x07060302u);
      }
      if (jp < 3) __builtin_amdgcn_sched_barrier(0);
    }
    {
      LAS char* p = L + L_OW + w * OW_W + (r & 3) * OW_H + gg * 16;
      f32x4 D[8];
#pragma unroll
      for (int e = 0; e < 8; ++e) D[e] = mfma(af[e], pf, f32x4{0.f, 0.f, 0.f, 0.f});
      __builtin_amdgcn_sched_barrier(0);
      if (r < 4) {
#pragma unroll
        for (int e = 0; e < 8; ++e) *(LAS f32x4*)(p + e * 64) = D[e];
      }
    }
    STAMP(7);
    BARRIER();
    WAITVM(0);
    lds_inc((unsigned)(size_t)(L + L_CNT + 8));
    while (__hip_atomic_load(cnt + 2, RLX_WG) < 4u) __builtin_amdgcn_s_sleep(1);
    STAMP(8);
    v4u bf[4];
    {
      const unsigned a0 = (unsigned)(size_t)(L + L_WO) + (8 * gg + (r >> 2)) * 256 + w * 64 + (r & 3) * 8;
      v2u t[8];
      asm volatile(
          "ds_read_b64_tr_b16 %0, %8\n\tds_read_b64_tr_b16 %1, %8 offset:1024\n\tds_read_b64_tr_b16 %2, %8 offset:8192\n\tds_read_b64_tr_b16 %3, %8 offset:9216\n\t"
          "ds_read_b64_tr_b16 %4, %8 offset:32\n\tds_read_b64_tr_b16 %5, %8 offset:1056\n\tds_read_b64_tr_b16 %6, %8 offset:8224\n\tds_read_b64_tr_b16 %7, %8 offset:9248\n\t"
          "s_waitcnt lgkmcnt(0)"
          : "=&v"(t[0]), "=&v"(t[1]), "=&v"(t[2]), "=&v"(t[3]), "=&v"(t[4]), "=&v"(t[5]), "=&v"(t[6]), "=&v"(t[7]) : "v"(a0) : "memory");
#pragma unroll
      for (int i = 0; i < 4; ++i) bf[i] = v4u{t[2 * i].x, t[2 * i].y, t[2 * i + 1].x, t[2 * i + 1].y};
    }
    BARRIER();
    STAMP(9);
    const v4u oa0 = *(const LAS v4u*)(L + L_OIMG + gg * 16), oa1 = *(const LAS v4u*)(L + L_OIMG + 64 + gg * 16);
    f32x4 D0 = mfma(oa0, bf[0], f32x4{0.f, 0.f, 0.f, 0.f}), D1 = mfma(oa0, bf[2], f32x4{0.f, 0.f, 0.f, 0.f});
    D0 = mfma(oa1, bf[1], D0); D1 = mfma(oa1, bf[3], D1);
    if (lane < 32) __hip_atomic_store((GAS unsigned*)(ws + WS_S + (cb * 64 + g * 8 + rb) * 512 + w * 128) + lane, nosent((lane & 16) ? D1.x : D0.x), RLX_AGENT);
    STAMP(10);
  }
}
}

extern "C" void kernel_launch(void* const* d_in, const int* in_sizes, int n_in, void* d_out, int out_size, void* d_ws, size_t ws_size, hipStream_t stream) {
  fused::fused_kernel<<<256, 768, 0, stream>>>((const char*)d_in[0], (const char*)d_in[1], (const char*)d_in[2], (const char*)d_in[3], (const char*)d_in[4],
                                               (const char*)d_in[6], (const char*)d_in[7], (const char*)d_in[8], (const char*)d_in[9], (unsigned short*)d_out, (char*)d_ws);
}
```

```cpp
#include <hip/hip_runtime.h>
#include <hip/hip_bf16.h>
#include <stdint.h>

namespace fused {
typedef unsigned long long u64;
typedef unsigned v4u __attribute__((ext_vector_type(4)));
typedef __bf16 bf16x2 __attribute__((ext_vector_type(2)));
typedef float f32x2 __attribute__((ext_vector_type(2)));
#define LAS __attribute__((address_space(3)))
constexpr int L_GATE = 64, L_WO = 3584, L_END = 57344;

__device__ __forceinline__ float u2f(unsigned u) { return __builtin_bit_cast(float, u); }
__device__ __forceinline__ unsigned f2u(float f) { return __builtin_bit_cast(unsigned, f); }
__device__ __forceinline__ float bflo(unsigned u) { return u2f(u << 16); }
__device__ __forceinline__ float bfhi(unsigned u) { return u2f(u & 0xffff0000u); }
__device__ __forceinline__ unsigned pkbf(float lo, float hi) { f32x2 v = {lo, hi}; return __builtin_bit_cast(unsigned, __builtin_convertvector(v, bf16x2)); }
__device__ __forceinline__ float rbf(float f) { return bflo(pkbf(f, f)); }
__device__ __forceinline__ void rbf2(float& a, float& b) { unsigned p = pkbf(a, b); a = bflo(p); b = bfhi(p); }
template <int CTRL> __device__ __forceinline__ float dpp(float x) { return u2f(__builtin_amdgcn_mov_dpp(f2u(x), CTRL, 0xf, 0xf, true)); }
__device__ __forceinline__ float sw32add(float a, float b) { auto r = __builtin_amdgcn_permlane32_swap(f2u(a), f2u(b), false, false); return u2f(r[0]) + u2f(r[1]); }
__device__ __forceinline__ float sw16add(float a, float b) { auto r = __builtin_amdgcn_permlane16_swap(f2u(a), f2u(b), false, false); return u2f(r[0]) + u2f(r[1]); }
__device__ __forceinline__ float rowsum(float v) { v += dpp<0x128>(v); v += dpp<0x124>(v); v += dpp<0x122>(v); v += dpp<0x121>(v); return v; }
__device__ __forceinline__ float wavesum(float v) { v = rowsum(v); v = sw16add(v, v); return sw32add(v, v); }

struct LN { float m, rs; };
__device__ __forceinline__ LN ln_stats(const float (&x)[8]) {
  float s = ((x[0] + x[1]) + (x[2] + x[3])) + ((x[4] + x[5]) + (x[6] + x[7]));
  const float mean = wavesum(s) * (1.0f / 512.0f);
  float q = 0.f;
#pragma unroll
  for (int i = 0; i < 8; ++i) { const float d = x[i] - mean; q = fmaf(d, d, q); }
  const float var = rbf(wavesum(q) * (1.0f / 512.0f));
  LN r; r.m = rbf(mean); r.rs = __builtin_amdgcn_rcpf(rbf(__builtin_amdgcn_sqrtf(rbf(var + 1e-5f))));
  return r;
}
__device__ __forceinline__ void ln_apply2(float& a, float& b, LN s) { a -= s.m; b -= s.m; rbf2(a, b); a *= s.rs; b *= s.rs; rbf2(a, b); }

struct Args { const char *x, *wq, *K, *w1, *b1, *w2; char* ws; const char *V, *wo, *b2; char* out; };

#define V10(n) "v" #n "0", "v" #n "1", "v" #n "2", "v" #n "3", "v" #n "4", "v" #n "5", "v" #n "6", "v" #n "7", "v" #n "8", "v" #n "9"
#define S10(n) "s" #n "0", "s" #n "1", "s" #n "2", "s" #n "3", "s" #n "4", "s" #n "5", "s" #n "6", "s" #n "7", "s" #n "8", "s" #n "9"

__global__ void __launch_bounds__(768, 3) fused_kernel(Args a) {
  __shared__ __attribute__((aligned(16))) char smem[L_END];
  LAS char* const L = (LAS char*)smem;
  auto ap = __builtin_amdgcn_kernarg_segment_ptr();
  int b = blockIdx.x, tid = threadIdx.x;
  asm volatile(
      "s_load_dwordx16 s[36:51], s[0:1], 0x0\n\t"
      "s_getpc_b64 s[58:59]\n\t"
      "v_readfirstlane_b32 s3, v0\n\t"
      "v_and_b32 v1, 63, v0\n\t"
      "s_and_b32 s5, s2, 7\n\t"
      "s_lshr_b32 s6, s2, 3\n\t"
      "s_bfe_u32 s4, s3, 0x20006\n\t"
      "v_lshlrev_b32 v2, 4, v1\n\t"
      "s_cmpk_ge_u32 s3, 0x100\n\t"
      "s_cbranch_scc1 .Lnotw\n\t"
      "v_mul_u32_u24 v3, 28, v0\n\t"
      "global_load_dword v95, v3, s[58:59]\n\t"
      "s_lshl_b32 s7, s6, 17\n\t"
      "s_and_b32 s7, s7, 0x300000\n\t"
      "s_lshl_b32 s8, s4, 16\n\t"
      "s_lshl_b32 s9, s5, 10\n\t"
      "s_or_b32 s7, s7, s8\n\t"
      "s_lshl_b32 s8, s6, 7\n\t"
      "s_and_b32 s8, s8, 0x380\n\t"
      "s_or_b32 s7, s7, s9\n\t"
      "s_or_b32 s7, s7, s8\n\t"
      "v_lshrrev_b32 v8, 3, v1\n\t"
      "v_and_b32 v4, 0x70, v2\n\t"
      "v_lshl_or_b32 v3, v8, 13, v4\n\t"
      "v_or_b32 v3, s7, v3\n\t"
      "s_lshl_b32 s7, s5, 5\n\t"
      "s_add_u32 s7, s7, s6\n\t"
      "s_mul_i32 s23, s7, 4224\n\t"
      "s_add_u32 s23, s23, 135168\n\t"
      "v_mov_b32 v14, 0\n\t"
      "v_mov_b32 v15, 0\n\t"
      "ds_write2_b64 v14, v[14:15], v[14:15] offset1:1\n\t"
      "ds_write2_b64 v14, v[14:15], v[14:15] offset0:2 offset1:3\n\t"
      "ds_write2_b64 v14, v[14:15], v[14:15] offset0:4 offset1:5\n\t"
      "s_setprio 3\n\t"
      "s_waitcnt lgkmcnt(0)\n\t"
      "s_add_u32 s8, s38, 0x40000\n\t"
      "s_addc_u32 s9, s39, 0\n\t"
      "global_load_dwordx4 v[108:111], v2, s[36:37]\n\t"
      "global_load_dwordx4 v[112:115], v3, s[38:39] nt\n\t"
      "s_add_u32 s38, s38, 0x80000\n\t"
      "s_addc_u32 s39, s39, 0\n\t"
      "global_load_dwordx4 v[116:119], v3, s[8:9] nt\n\t"
      "s_add_u32 s8, s8, 0x80000\n\t"
      "s_addc_u32 s9, s9, 0\n\t"
      "global_load_dwordx4 v[120:123], v3, s[38:39] nt\n\t"
      "global_load_dwordx4 v[124:127], v3, s[8:9] nt\n\t"
      "global_load_dwordx2 v[104:105], v14, s[48:49] sc1\n\t"
      "s_barrier\n\t"
      "s_branch .Lwmain\n\t"
      ".Lnotw:\n\t"
      "s_lshl_b32 s8, s4, 10\n\t"
      "s_cmpk_lt_u32 s3, 0x200\n\t"
      "s_cbranch_scc1 .Lgpro\n\t"
      "s_lshl_b32 s7, s5, 23\n\t"
      "s_lshl_b32 s9, s6, 10\n\t"
      "s_add_u32 s7, s7, s9\n\t"
      "s_add_u32 s7, s7, 0x700000\n\t"
      "s_lshl_b32 s8, s4, 15\n\t"
      "s_add_u32 s7, s7, s8\n\t"
      "v_bfe_u32 v3, v1, 2, 2\n\t"
      "v_and_b32 v4, 0x30, v2\n\t"
      "v_and_b32 v5, 0x30, v1\n\t"
      "v_lshl_or_b32 v3, v3, 8, v4\n\t"
      "v_lshl_or_b32 v3, v5, 2, v3\n\t"
      "v_add_u32 v3, s7, v3\n\t"
      "v_and_b32 v6, 16, v1\n\t"
      "v_lshlrev_b32 v6, 3, v6\n\t"
      "s_and_b32 s9, s4, 1\n\t"
      "s_lshl_b32 s9, s9, 6\n\t"
      "v_or_b32 v6, s9, v6\n\t"
      "v_xor_b32 v6, v6, v2\n\t"
      "v_add_u32 v6, s7, v6\n\t"
      "v_add_u32 v4, 0x2000, v3\n\t"
      "v_add_u32 v5, 0x4000, v3\n\t"
      "s_mov_b32 s14, 0\n\t"
      "s_setprio 2\n\t"
      "s_barrier\n\t"
      "s_waitcnt lgkmcnt(0)\n\t"
      "s_add_u32 s8, s40, 0x1000\n\t"
      "s_addc_u32 s9, s41, 0\n\t"
      "s_sleep 3\n\t"
      "s_add_u32 s12, s40, s14\n\t"
      "s_addc_u32 s13, s41, 0\n\t"
      "s_add_u32 s14, s14, 131072\n\t"
      "s_and_b32 s14, s14, 917504\n\t"
      "global_load_dwordx4 v[112:115], v3, s[12:13] nt\n\t"
      "s_add_u32 s12, s40, s14\n\t"
      "s_addc_u32 s13, s41, 0\n\t"
      "s_add_u32 s14, s14, 131072\n\t"
      "s_and_b32 s14, s14, 917504\n\t"
      "global_load_dwordx4 v[116:119], v3, s[12:13] nt\n\t"
      "s_add_u32 s12, s40, s14\n\t"
      "s_addc_u32 s13, s41, 0\n\t"
      "s_add_u32 s14, s14, 131072\n\t"
      "s_and_b32 s14, s14, 917504\n\t"
      "global_load_dwordx4 v[120:123], v3, s[12:13] nt\n\t"
      "s_add_u32 s12, s40, s14\n\t"
      "s_addc_u32 s13, s41, 0\n\t"
      "s_add_u32 s14, s14, 131072\n\t"
      "s_and_b32 s14, s14, 917504\n\t"
      "global_load_dwordx4 v[124:127], v3, s[12:13] nt\n\t"
      "s_branch .Lkvmain\n\t"
      ".Lgpro:\n\t"
      "v_add_u32 v3, s8, v2\n\t"
      "v_min_u32 v3, 0xdf0, v3\n\t"
      "s_barrier\n\t"
      "s_waitcnt lgkmcnt(0)\n\t"
      "s_load_dwordx4 s[52:55], s[0:1], 0x40\n\t"
      "global_load_dwordx4 v[108:111], v2, s[36:37]\n\t"
      "global_load_dwordx4 v[112:115], v3, s[42:43]\n\t"
      "global_load_dwordx4 v[116:119], v3, s[44:45]\n\t"
      "global_load_dwordx4 v[120:123], v3, s[46:47]\n\t"
      "v_mov_b32 v14, 0\n\t"
      "global_load_dword v104, v14, s[48:49] sc1\n\t"
      "s_lshl_b32 s7, s5, 5\n\t"
      "s_add_u32 s7, s7, s6\n\t"
      "s_mul_i32 s7, s7, 4224\n\t"
      "s_add_u32 s7, s7, 135168\n\t"
      "s_add_u32 s28, s48, s7\n\t"
      "s_addc_u32 s29, s49, 0\n\t"
      "s_add_u32 s7, s7, s8\n\t"
      "v_add_u32 v4, s7, v2\n\t"
      "s_lshl_b32 s7, s5, 11\n\t"
      "s_lshl_b32 s9, s6, 4\n\t"
      "s_add_u32 s7, s7, s9\n\t"
      "s_add_u32 s7, s7, 1216512\n\t"
      "v_lshl_add_u32 v5, v1, 9, s7\n\t"
      "s_cmp_eq_u32 s4, 3\n\t"
      "s_cselect_b64 vcc, -1, 0\n\t"
      "v_mov_b32 v96, -1\n\t"
      "v_mov_b32 v97, -1\n\t"
      "v_cndmask_b32 v4, v4, v5, vcc\n\t"
      "v_mov_b32 v98, -1\n\t"
      "v_mov_b32 v99, -1\n\t"
      "s_cselect_b64 exec, 15, -1\n\t"
      "global_store_dwordx4 v4, v[96:99], s[48:49] sc1\n\t"
      "s_mov_b64 exec, -1\n\t"
      "s_setprio 0\n\t"
      "s_branch .Lgmain\n\t"
      ".Lwmain:\n\t"
      "s_lshl_b32 s7, s4, 10\n\t"
      "s_add_u32 s7, s7, 19968\n\t"
      "v_add_u32 v5, s7, v2\n\t"
      "s_lshr_b32 s8, s6, 3\n\t"
      "s_lshl_b32 s8, s8, 8\n\t"
      "s_lshl_b32 s9, s4, 4\n\t"
      "s_add_u32 s8, s8, s9\n\t"
      "s_add_u32 s8, s8, s7\n\t"
      "v_lshl_add_u32 v9, v8, 1, s8\n\t"
      "v_and_b32 v11, 7, v1\n\t"
      "v_lshlrev_b32 v11, 5, v11\n\t"
      "v_lshl_add_u32 v11, v8, 2, v11\n\t"
      "s_lshl_b32 s11, s4, 8\n\t"
      "s_add_u32 s11, s11, 512\n\t"
      "v_add_u32 v11, s11, v11\n\t"
      "s_lshl_b32 s14, s5, 14\n\t"
      "s_lshl_b32 s15, s6, 9\n\t"
      "s_add_u32 s14, s14, s15\n\t"
      "s_add_u32 s14, s14, 4096\n\t"
      "s_add_u32 s12, s48, s14\n\t"
      "s_addc_u32 s13, s49, 0\n\t"
      "v_lshlrev_b32 v12, 3, v1\n\t"
      "v_lshlrev_b32 v13, 2, v1\n\t"
      "v_mov_b32 v15, 1\n\t"
      "v_mov_b32 v43, 0x3b000000\n\t"
      "s_mul_i32 s7, s5, 135168\n\t"
      "s_add_u32 s7, s7, 135168\n\t"
      "s_add_u32 s46, s48, s7\n\t"
      "s_addc_u32 s47, s49, 0\n\t"
      "v_mul_u32_u24 v67, 4224, v1\n\t"
      "v_add_u32 v67, 2688, v67\n\t"
      "v_and_b32 v16, 8, v1\n\t"
      "v_cmp_ne_u32 s[16:17], 0, v16\n\t"
      "s_waitcnt vmcnt(5)\n\t"
      "ds_write_b128 v5, v[108:111]\n\t"
      "v_lshlrev_b32 v30, 16, v108\n\t"
      "v_and_b32 v31, 0xffff0000, v108\n\t"
      "v_readfirstlane_b32 s18, v30\n\t"
      "ds_read_u16 v20, v9\n\t"
      "ds_read_u16 v21, v9 offset:64\n\t"
      "ds_read_u16 v22, v9 offset:128\n\t"
      "ds_read_u16 v23, v9 offset:192\n\t"
      "v_lshlrev_b32 v32, 16, v109\n\t"
      "v_and_b32 v33, 0xffff0000, v109\n\t"
      "v_lshlrev_b32 v34, 16, v110\n\t"
      "v_and_b32 v35, 0xffff0000, v110\n\t"
      "v_lshlrev_b32 v36, 16, v111\n\t"
      "v_and_b32 v37, 0xffff0000, v111\n\t"
      "v_pk_add_f32 v[30:31], v[30:31], s[18:19] op_sel_hi:[1,0] neg_lo:[0,1] neg_hi:[0,1]\n\t"
      "v_pk_add_f32 v[32:33], v[32:33], s[18:19] op_sel_hi:[1,0] neg_lo:[0,1] neg_hi:[0,1]\n\t"
      "v_pk_add_f32 v[34:35], v[34:35], s[18:19] op_sel_hi:[1,0] neg_lo:[0,1] neg_hi:[0,1]\n\t"
      "v_pk_add_f32 v[36:37], v[36:37], s[18:19] op_sel_hi:[1,0] neg_lo:[0,1] neg_hi:[0,1]\n\t"
      "v_pk_add_f32 v[38:39], v[30:31], v[32:33]\n\t"
      "v_pk_mul_f32 v[40:41], v[30:31], v[30:31]\n\t"
      "v_pk_add_f32 v[38:39], v[38:39], v[34:35]\n\t"
      "v_pk_fma_f32 v[40:41], v[32:33], v[32:33], v[40:41]\n\t"
      "v_pk_add_f32 v[38:39], v[38:39], v[36:37]\n\t"
      "v_pk_fma_f32 v[40:41], v[34:35], v[34:35], v[40:41]\n\t"
      "v_add_f32 v38, v38, v39\n\t"
      "v_pk_fma_f32 v[40:41], v[36:37], v[36:37], v[40:41]\n\t"
      "s_waitcnt lgkmcnt(0)\n\t"
      "v_add_f32 v40, v40, v41\n\t"
      "v_add_f32_dpp v38, v38, v38 row_ror:8 row_mask:0xf bank_mask:0xf bound_ctrl:1\n\t"
      "v_lshlrev_b32 v20, 16, v20\n\t"
      "v_add_f32_dpp v40, v40, v40 row_ror:8 row_mask:0xf bank_mask:0xf bound_ctrl:1\n\t"
      "v_add_f32_dpp v38, v38, v38 row_ror:4 row_mask:0xf bank_mask:0xf bound_ctrl:1\n\t"
      "v_lshlrev_b32 v21, 16, v21\n\t"
      "v_add_f32_dpp v40, v40, v40 row_ror:4 row_mask:0xf bank_mask:0xf bound_ctrl:1\n\t"
      "v_add_f32_dpp v38, v38, v38 row_ror:2 row_mask:0xf bank_mask:0xf bound_ctrl:1\n\t"
      "v_lshlrev_b32 v22, 16, v22\n\t"
      "v_add_f32_dpp v40, v40, v40 row_ror:2 row_mask:0xf bank_mask:0xf bound_ctrl:1\n\t"
      "v_add_f32_dpp v38, v38, v38 row_ror:1 row_mask:0xf bank_mask:0xf bound_ctrl:1\n\t"
      "v_lshlrev_b32 v23, 16, v23\n\t"
      "v_add_f32_dpp v40, v40, v40 row_ror:1 row_mask:0xf bank_mask:0xf bound_ctrl:1\n\t"
      "v_add_f32_dpp v38, v38, v38 row_bcast:15 row_mask:0xa bank_mask:0xf\n\t"
      "s_nop 0\n\t"
      "v_add_f32_dpp v40, v40, v40 row_bcast:15 row_mask:0xa bank_mask:0xf\n\t"
      "v_add_f32_dpp v38, v38, v38 row_bcast:31 row_mask:0xc bank_mask:0xf\n\t"
      "s_nop 0\n\t"
      "v_add_f32_dpp v40, v40, v40 row_bcast:31 row_mask:0xc bank_mask:0xf\n\t"
      "v_readlane_b32 s20, v38, 63\n\t"
      "v_readlane_b32 s21, v40, 63\n\t"
      "s_nop 0\n\t"
      "v_mul_f32 v42, s20, v43\n\t"
      "v_mul_f32 v44, s21, v43\n\t"
      "v_fma_f32 v44, -v42, v42, v44\n\t"
      "v_add_f32 v42, s18, v42\n\t"
      "v_cvt_pk_bf16_f32 v45, v42, v44\n\t"
      "v_and_b32 v44, 0xffff0000, v45\n\t"
      "v_add_f32 v44, 0x3727c5ac, v44\n\t"
      "v_cvt_pk_bf16_f32 v44, v44, v44\n\t"
      "v_lshlrev_b32 v44, 16, v44\n\t"
      "v_sqrt_f32 v44, v44\n\t"
      "v_lshlrev_b32 v42, 16, v45\n\t"
      "v_cvt_pk_bf16_f32 v44, v44, v44\n\t"
      "v_lshlrev_b32 v44, 16, v44\n\t"
      "v_rcp_f32 v46, v44\n\t"
      "v_pk_add_f32 v[20:21], v[20:21], v[42:43] op_sel_hi:[1,0] neg_lo:[0,1] neg_hi:[0,1]\n\t"
      "v_pk_add_f32 v[22:23], v[22:23], v[42:43] op_sel_hi:[1,0] neg_lo:[0,1] neg_hi:[0,1]\n\t"
      "v_cvt_pk_bf16_f32 v21, v20, v21\n\t"
      "v_cvt_pk_bf16_f32 v23, v22, v23\n\t"
      "v_lshlrev_b32 v20, 16, v21\n\t"
      "v_and_b32 v21, 0xffff0000, v21\n\t"
      "v_lshlrev_b32 v22, 16, v23\n\t"
      "v_and_b32 v23, 0xffff0000, v23\n\t"
      "v_pk_mul_f32 v[20:21], v[20:21], v[46:47] op_sel_hi:[1,0]\n\t"
      "v_pk_mul_f32 v[22:23], v[22:23], v[46:47] op_sel_hi:[1,0]\n\t"
      "v_cvt_pk_bf16_f32 v21, v20, v21\n\t"
      "v_cvt_pk_bf16_f32 v23, v22, v23\n\t"
      "v_lshlrev_b32 v20, 16, v21\n\t"
      "v_and_b32 v21, 0xffff0000, v21\n\t"
      "v_lshlrev_b32 v22, 16, v23\n\t"
      "v_and_b32 v23, 0xffff0000, v23\n\t"
      "s_waitcnt vmcnt(4)\n\t"
      "v_lshlrev_b32 v48, 16, v112\n\t"
      "v_and_b32 v49, 0xffff0000, v112\n\t"
      "v_lshlrev_b32 v50, 16, v113\n\t"
      "v_and_b32 v51, 0xffff0000, v113\n\t"
      "v_lshlrev_b32 v52, 16, v114\n\t"
      "v_and_b32 v53, 0xffff0000, v114\n\t"
      "v_lshlrev_b32 v54, 16, v115\n\t"
      "v_and_b32 v55, 0xffff0000, v115\n\t"
      "v_pk_mul_f32 v[56:57], v[48:49], v[20:21] op_sel:[0,0] op_sel_hi:[1,0]\n\t"
      "v_pk_mul_f32 v[58:59], v[50:51], v[20:21] op_sel:[0,0] op_sel_hi:[1,0]\n\t"
      "v_pk_mul_f32 v[60:61], v[52:53], v[20:21] op_sel:[0,0] op_sel_hi:[1,0]\n\t"
      "v_pk_mul_f32 v[62:63], v[54:55], v[20:21] op_sel:[0,0] op_sel_hi:[1,0]\n\t"
      "s_waitcnt vmcnt(3)\n\t"
      "v_lshlrev_b32 v48, 16, v116\n\t"
      "v_and_b32 v49, 0xffff0000, v116\n\t"
      "v_lshlrev_b32 v50, 16, v117\n\t"
      "v_and_b32 v51, 0xffff0000, v117\n\t"
      "v_lshlrev_b32 v52, 16, v118\n\t"
      "v_and_b32 v53, 0xffff0000, v118\n\t"
      "v_lshlrev_b32 v54, 16, v119\n\t"
      "v_and_b32 v55, 0xffff0000, v119\n\t"
      "v_pk_fma_f32 v[56:57], v[48:49], v[20:21], v[56:57] op_sel:[0,1,0] op_sel_hi:[1,1,1]\n\t"
      "v_pk_fma_f32 v[58:59], v[50:51], v[20:21], v[58:59] op_sel:[0,1,0] op_sel_hi:[1,1,1]\n\t"
      "v_pk_fma_f32 v[60:61], v[52:53], v[20:21], v[60:61] op_sel:[0,1,0] op_sel_hi:[1,1,1]\n\t"
      "v_pk_fma_f32 v[62:63], v[54:55], v[20:21], v[62:63] op_sel:[0,1,0] op_sel_hi:[1,1,1]\n\t"
      "s_waitcnt vmcnt(2)\n\t"
      "v_lshlrev_b32 v48, 16, v120\n\t"
      "v_and_b32 v49, 0xffff0000, v120\n\t"
      "v_lshlrev_b32 v50, 16, v121\n\t"
      "v_and_b32 v51, 0xffff0000, v121\n\t"
      "v_lshlrev_b32 v52, 16, v122\n\t"
      "v_and_b32 v53, 0xffff0000, v122\n\t"
      "v_lshlrev_b32 v54, 16, v123\n\t"
      "v_and_b32 v55, 0xffff0000, v123\n\t"
      "v_pk_fma_f32 v[56:57], v[48:49], v[22:23], v[56:57] op_sel:[0,0,0] op_sel_hi:[1,0,1]\n\t"
      "v_pk_fma_f32 v[58:59], v[50:51], v[22:23], v[58:59] op_sel:[0,0,0] op_sel_hi:[1,0,1]\n\t"
      "v_pk_fma_f32 v[60:61], v[52:53], v[22:23], v[60:61] op_sel:[0,0,0] op_sel_hi:[1,0,1]\n\t"
      "v_pk_fma_f32 v[62:63], v[54:55], v[22:23], v[62:63] op_sel:[0,0,0] op_sel_hi:[1,0,1]\n\t"
      "s_waitcnt vmcnt(1)\n\t"
      "v_lshlrev_b32 v48, 16, v124\n\t"
      "v_and_b32 v49, 0xffff0000, v124\n\t"
      "v_lshlrev_b32 v50, 16, v125\n\t"
      "v_and_b32 v51, 0xffff0000, v125\n\t"
      "v_lshlrev_b32 v52, 16, v126\n\t"
      "v_and_b32 v53, 0xffff0000, v126\n\t"
      "v_lshlrev_b32 v54, 16, v127\n\t"
      "v_and_b32 v55, 0xffff0000, v127\n\t"
      "v_pk_fma_f32 v[56:57], v[48:49], v[22:23], v[56:57] op_sel:[0,1,0] op_sel_hi:[1,1,1]\n\t"
      "v_pk_fma_f32 v[58:59], v[50:51], v[22:23], v[58:59] op_sel:[0,1,0] op_sel_hi:[1,1,1]\n\t"
      "v_pk_fma_f32 v[60:61], v[52:53], v[22:23], v[60:61] op_sel:[0,1,0] op_sel_hi:[1,1,1]\n\t"
      "v_pk_fma_f32 v[62:63], v[54:55], v[22:23], v[62:63] op_sel:[0,1,0] op_sel_hi:[1,1,1]\n\t"
      "s_waitcnt vmcnt(0)\n\t"
      "v_add_u32 v27, 1, v104\n\t"
      "v_permlane32_swap_b32 v56, v60\n\t"
      "v_permlane32_swap_b32 v57, v61\n\t"
      "v_permlane32_swap_b32 v58, v62\n\t"
      "v_permlane32_swap_b32 v59, v63\n\t"
      "v_add_f32 v56, v56, v60\n\t"
      "v_add_f32 v58, v58, v62\n\t"
      "v_add_f32 v57, v57, v61\n\t"
      "v_add_f32 v59, v59, v63\n\t"
      "v_permlane16_swap_b32 v56, v58\n\t"
      "s_nop 0\n\t"
      "v_permlane16_swap_b32 v57, v59\n\t"
      "v_add_f32 v56, v56, v58\n\t"
      "v_add_f32 v57, v57, v59\n\t"
      "s_nop 0\n\t"
      "v_add_f32_dpp v56, v56, v56 row_ror:8 row_mask:0xf bank_mask:0xf bound_ctrl:1\n\t"
      "v_add_f32_dpp v57, v57, v57 row_ror:8 row_mask:0xf bank_mask:0xf bound_ctrl:1\n\t"
      "v_cndmask_b32 v56, v56, v57, s[16:17]\n\t"
      "ds_write_b32 v11, v56\n\t"
      "s_mov_b64 exec, 1\n\t"
      "ds_add_rtn_u32 v17, v14, v15\n\t"
      "s_mov_b64 exec, -1\n\t"
      "ds_read2st64_b32 v[18:19], v13 offset0:2 offset1:3\n\t"
      "ds_read2st64_b32 v[28:29], v13 offset0:4 offset1:5\n\t"
      "s_waitcnt lgkmcnt(0)\n\t"
      "v_readfirstlane_b32 s22, v17\n\t"
      "v_pk_add_f32 v[18:19], v[18:19], v[28:29]\n\t"
      "s_cmp_lg_u32 s22, 3\n\t"
      "v_add_f32 v26, v18, v19\n\t"
      "s_cbranch_scc1 .Lwnopub\n\t"
      "global_store_dwordx2 v12, v[26:27], s[12:13] sc1\n\t"
      ".Lwnopub:\n\t"
      "s_setprio 1\n\t"
      "s_add_u32 s26, s48, s23\n\t"
      "s_addc_u32 s27, s49, 0\n\t"
      "s_mov_b64 s[36:37], s[46:47]\n\t"
      "s_lshr_b32 s10, s6, 3\n\t"
      "s_lshl_b32 s7, s5, 11\n\t"
      "s_add_u32 s7, s7, 1216512\n\t"
      "s_add_u32 s24, s48, s7\n\t"
      "s_addc_u32 s25, s49, 0\n\t"
      "s_lshl_b32 s7, s6, 4\n\t"
      "v_lshl_add_u32 v29, v1, 9, s7\n\t"
      "v_and_b32 v34, 3, v1\n\t"
      "v_lshlrev_b32 v30, 7, v34\n\t"
      "v_lshl_or_b32 v30, v8, 4, v30\n\t"
      "s_lshl_b32 s7, s10, 9\n\t"
      "v_add_u32 v30, s7, v30\n\t"
      "s_lshr_b32 s11, s6, 2\n\t"
      "s_lshl_b32 s11, s11, 8\n\t"
      "s_and_b32 s7, s4, 1\n\t"
      "s_lshl_b32 s7, s7, 7\n\t"
      "s_add_u32 s11, s11, s7\n\t"
      "s_add_u32 s16, s36, s11\n\t"
      "s_addc_u32 s17, s37, 0\n\t"
      "s_add_u32 s18, s16, 33792\n\t"
      "s_addc_u32 s19, s17, 0\n\t"
      "v_mul_u32_u24 v31, 4224, v8\n\t"
      "v_and_b32 v65, 0x70, v2\n\t"
      "v_add_u32 v31, v31, v65\n\t"
      "v_add_u32 v28, 67584, v31\n\t"
      "v_lshrrev_b32 v36, 5, v1\n\t"
      "v_bfe_u32 v37, v1, 4, 1\n\t"
      "v_lshl_or_b32 v64, v36, 1, v37\n\t"
      "v_lshrrev_b32 v37, 1, v65\n\t"
      "v_lshl_or_b32 v37, v64, 1, v37\n\t"
      "s_lshr_b32 s7, s7, 1\n\t"
      "s_add_u32 s7, s7, 256\n\t"
      "v_add_u32 v37, s7, v37\n\t"
      "s_lshr_b32 s7, s5, 1\n\t"
      "s_mul_i32 s11, s7, 4224\n\t"
      "s_add_u32 s11, s11, 135168\n\t"
      "s_add_u32 s38, s48, s11\n\t"
      "s_addc_u32 s39, s49, 0\n\t"
      "s_and_b32 s22, s5, 1\n\t"
      "s_lshl_b32 s22, s22, 8\n\t"
      "s_lshl_b32 s11, s6, 6\n\t"
      "s_add_u32 s11, s11, s22\n\t"
      "s_add_u32 s11, s11, 2048\n\t"
      "s_add_u32 s62, s38, s11\n\t"
      "s_addc_u32 s63, s39, 0\n\t"
      "s_add_u32 s40, s62, 270336\n\t"
      "s_addc_u32 s41, s63, 0\n\t"
      "v_lshrrev_b32 v33, 2, v1\n\t"
      "v_mul_u32_u24 v33, 16896, v33\n\t"
      "v_and_b32 v65, 0x30, v2\n\t"
      "v_add_u32 v33, v33, v65\n\t"
      "v_add_u32 v66, 540672, v33\n\t"
      "v_mul_u32_u24 v32, 16896, v1\n\t"
      "v_add_u32 v32, 3072, v32\n\t"
      "v_lshlrev_b32 v35, 4, v34\n\t"
      "v_mul_u32_u24 v68, 135168, v1\n\t"
      "s_lshl_b32 s11, s5, 2\n\t"
      "s_add_u32 s11, s11, s6\n\t"
      "s_and_b32 s11, s11, 7\n\t"
      "s_mul_i32 s11, s11, 16896\n\t"
      "s_add_u32 s11, s11, 2816\n\t"
      "v_add_u32 v68, s11, v68\n\t"
      "s_and_b32 s11, s6, 3\n\t"
      "s_mul_i32 s11, s11, 4224\n\t"
      "s_add_u32 s11, s11, -256\n\t"
      "v_add_u32 v69, s11, v32\n\t"
      "v_lshl_or_b32 v70, v34, 2, v64\n\t"
      "s_lshl_b32 s11, s6, 3\n\t"
      "v_lshrrev_b32 v71, 1, v70\n\t"
      "v_add_u32 v71, s11, v71\n\t"
      "v_mul_u32_u24 v71, 4224, v71\n\t"
      "v_and_b32 v72, 1, v70\n\t"
      "v_lshl_add_u32 v71, v72, 2, v71\n\t"
      "v_add_u32 v71, 2560, v71\n\t"
      "v_lshlrev_b32 v40, 1, v70\n\t"
      "s_lshl_b32 s7, s5, 7\n\t"
      "s_lshl_b32 s11, s6, 5\n\t"
      "s_add_u32 s7, s7, s11\n\t"
      "s_mov_b32 s61, s7\n\t"
      "s_add_u32 s7, s7, 19968\n\t"
      "v_add_u32 v72, s7, v40\n\t"
      "ds_read_u16 v41, v72\n\t"
      "v_lshlrev_b32 v39, 2, v36\n\t"
      "v_mov_b32 v44, 0x3fb8aa3b\n\t"
      "s_and_b32 s7, s6, 15\n\t"
      "s_lshl_b64 s[42:43], 1, s7\n\t"
      "v_mov_b32 v74, 0\n\t"
      "v_mov_b32 v75, 0\n\t"
      "v_mov_b32 v76, 0\n\t"
      "v_mov_b32 v77, 0\n\t"
      "v_mov_b32 v96, v104\n\t"
      "v_mov_b32 v97, -1\n\t"
      "v_mov_b32 v82, v104\n\t"
      "s_waitcnt lgkmcnt(0)\n\t"
      "v_lshlrev_b32 v41, 16, v41\n\t"
      "s_lshl_b32 s8, s5, 14\n\t"
      "s_lshl_b32 s9, s4, 10\n\t"
      "s_add_u32 s8, s8, s9\n\t"
      "s_add_u32 s8, s8, 4096\n\t"
      "s_add_u32 s8, s48, s8\n\t"
      "s_addc_u32 s9, s49, 0\n\t"
      "s_add_u32 s52, s8, 0x1000\n\t"
      "s_addc_u32 s53, s9, 0\n\t"
      "v_add_u32 v7, 0x2000, v2\n\t"
      "s_lshl_b32 s7, s4, 8\n\t"
      "s_add_u32 s7, s7, 1536\n\t"
      "v_add_u32 v13, s7, v13\n\t"
      ".Lwh1y:\n\t"
      "ds_read_b32 v46, v14 offset:16\n\t"
      "s_sleep 1\n\t"
      "s_waitcnt lgkmcnt(0)\n\t"
      "v_cmp_gt_u32 vcc, 4, v46\n\t"
      "s_cbranch_vccnz .Lwh1y\n\t"
      "s_setprio 3\n\t"
      "s_mov_b32 s7, 65536\n\t"
      ".Lwq:\n\t"
      "global_load_dwordx4 v[84:87], v2, s[8:9] sc1\n\t"
      "global_load_dwordx4 v[88:91], v2, s[52:53] sc1\n\t"
      "global_load_dwordx4 v[92:95], v7, s[8:9] sc1\n\t"
      "global_load_dwordx4 v[48:51], v7, s[52:53] sc1\n\t"
      "s_mov_b64 exec, 1\n\t"
      "ds_add_u32 v14, v15 offset:24\n\t"
      "s_mov_b64 exec, -1\n\t"
      "s_waitcnt vmcnt(0)\n\t"
      "v_max3_u32 v52, v85, v87, v89\n\t"
      "v_min3_u32 v53, v85, v87, v89\n\t"
      "v_max3_u32 v52, v52, v91, v93\n\t"
      "v_min3_u32 v53, v53, v91, v93\n\t"
      "v_max3_u32 v52, v52, v95, v49\n\t"
      "v_min3_u32 v53, v53, v95, v49\n\t"
      "v_max3_u32 v52, v52, v51, v27\n\t"
      "v_min3_u32 v53, v53, v51, v27\n\t"
      "v_add_f32 v54, v84, v88\n\t"
      "v_add_f32 v55, v86, v90\n\t"
      "v_cmp_ne_u32 vcc, v52, v53\n\t"
      "v_add_f32 v56, v92, v48\n\t"
      "v_add_f32 v57, v94, v50\n\t"
      "s_cbranch_vccz .Lwqd\n\t"
      "s_sub_u32 s7, s7, 1\n\t"
      "s_cbranch_scc0 .Lwq\n\t"
      ".Lwqd:\n\t"
      "v_pk_add_f32 v[54:55], v[54:55], v[56:57]\n\t"
      "v_cvt_pk_bf16_f32 v54, v54, v55\n\t"
      "ds_write_b32 v13, v54\n\t"
      "s_mov_b64 exec, 1\n\t"
      "ds_add_u32 v14, v15 offset:8\n\t"
      "s_mov_b64 exec, -1\n\t"
      "s_cmp_gt_u32 s4, 1\n\t"
      "s_cbranch_scc1 .Lw23\n\t"
      "s_cmp_eq_u32 s4, 1\n\t"
      "s_cbranch_scc1 .Lw1\n\t"
      "s_mov_b32 exec_hi, 0\n\t"
      "global_load_dword v82, v67, s[36:37] offset:384 sc1\n\t"
      "s_mov_b32 exec_hi, -1\n\t"
      "s_load_dwordx2 s[56:57], s[0:1], 0x50\n\t"
      "s_sleep 6\n\t"
      ".Lwml:\n\t"
      "ds_read_b128 v[56:59], v14 offset:32\n\t"
      "ds_read_b128 v[48:51], v35 offset:128\n\t"
      "ds_read_b128 v[52:55], v35 offset:192\n\t"
      "s_waitcnt lgkmcnt(0)\n\t"
      "v_min_u32 v56, v57, v58\n\t"
      "v_cmp_gt_u32 vcc, 4, v56\n\t"
      "s_cbranch_vccnz .Lwml\n\t"
      "s_add_u32 s56, s56, s61\n\t"
      "s_addc_u32 s57, s57, 0\n\t"
      "v_max3_f32 v80, v48, v49, v50\n\t"
      "v_pk_add_f32 v[52:53], v[52:53], v[54:55]\n\t"
      "v_max_f32 v80, v80, v51\n\t"
      "v_add_f32 v81, v52, v53\n\t"
      "s_mov_b64 exec, 15\n\t"
      "global_store_dwordx2 v29, v[80:81], s[24:25] sc1\n\t"
      "s_mov_b64 exec, -1\n\t"
      ".Lwv:\n\t"
      "ds_read_b32 v46, v14 offset:12\n\t"
      "s_waitcnt lgkmcnt(0)\n\t"
      "v_cmp_gt_u32 vcc, 4, v46\n\t"
      "s_cbranch_vccnz .Lwv\n\t"
      "s_mov_b32 exec_lo, 0xffff\n\t"
      "s_mov_b32 exec_hi, 0\n\t"
      "global_atomic_add v74, v67, v15, s[36:37] sc0 sc1\n\t"
      "s_mov_b32 exec_lo, -1\n\t"
      "s_mov_b32 s7, 65536\n\t"
      ".Lwc:\n\t"
      "v_cmp_ne_u32 vcc, v27, v82\n\t"
      "s_cbranch_vccz .Lwcd\n\t"
      "global_load_dword v82, v67, s[36:37] offset:384 sc1\n\t"
      "s_waitcnt vmcnt(0)\n\t"
      "s_sub_u32 s7, s7, 1\n\t"
      "s_cbranch_scc0 .Lwc\n\t"
      ".Lwcd:\n\t"
      "s_mov_b64 exec, s[42:43]\n\t"
      "s_mov_b32 s7, 100\n\t"
      ".Lwh2l:\n\t"
      "global_atomic_add v75, v67, v14, s[36:37] sc0 sc1\n\t"
      "s_mov_b32 s8, 2\n\t"
      ".Lwh2c:\n\t"
      "v_cmp_eq_u32 vcc, 30, v74\n\t"
      "v_cmp_eq_u32 s[10:11], 31, v75\n\t"
      "s_or_b64 vcc, vcc, s[10:11]\n\t"
      "s_cbranch_vccnz .Lwh2e\n\t"
      "s_sub_u32 s8, s8, 1\n\t"
      "s_cbranch_scc0 .Lwh2c\n\t"
      "s_sub_u32 s7, s7, 1\n\t"
      "s_cbranch_scc0 .Lwh2l\n\t"
      ".Lwh2e:\n\t"
      "s_mov_b64 exec, -1\n\t"
      "s_cmp_ge_u32 s6, 4\n\t"
      "s_barrier\n\t"
      "s_cbranch_scc1 .Lwb3\n\t"
      "s_sleep 3\n\t"
      "global_load_dword v96, v32, s[38:39] sc1\n\t"
      "global_load_dword v97, v71, s[36:37] sc1\n\t"
      "s_mov_b64 exec, 0xff\n\t"
      "s_mov_b32 s7, 200\n\t"
      "s_barrier\n\t"
      "global_atomic_add v76, v68, v14, s[38:39] sc0 sc1\n\t"
      ".Lwh3l:\n\t"
      "s_mov_b32 s8, 2\n\t"
      ".Lwh3c:\n\t"
      "v_cmp_ne_u32 vcc, 7, v76\n\t"
      "s_cbranch_vccz .Lwh3e\n\t"
      "v_cmp_ne_u32 vcc, 7, v77\n\t"
      "s_cbranch_vccz .Lwh3f\n\t"
      "s_sub_u32 s8, s8, 1\n\t"
      "s_cbranch_scc0 .Lwh3c\n\t"
      "global_atomic_add v77, v68, v14, s[38:39] sc0 sc1\n\t"
      "s_sub_u32 s7, s7, 1\n\t"
      "s_cbranch_scc0 .Lwh3l\n\t"
      ".Lwh3f:\n\t"
      ".Lwh3e:\n\t"
      "s_mov_b64 exec, -1\n\t"
      "s_mov_b32 s7, 65536\n\t"
      ".Lwt:\n\t"
      "v_cmp_ne_u32 vcc, v27, v96\n\t"
      "v_cmp_eq_u32 s[10:11], -1, v97\n\t"
      "s_or_b64 vcc, vcc, s[10:11]\n\t"
      "s_cbranch_vccz .Lwtd\n\t"
      "global_load_dword v96, v32, s[38:39] sc1\n\t"
      "global_load_dword v97, v71, s[36:37] sc1\n\t"
      "s_waitcnt vmcnt(0)\n\t"
      "s_sub_u32 s7, s7, 1\n\t"
      "s_cbranch_scc0 .Lwt\n\t"
      ".Lwtd:\n\t"
      "s_mov_b32 s7, 65536\n\t"
      ".Lwp3:\n\t"
      "global_load_dwordx4 v[84:87], v33, s[62:63] sc1\n\t"
      "global_load_dwordx4 v[88:91], v33, s[40:41] sc1\n\t"
      "global_load_dwordx4 v[48:51], v66, s[62:63] sc1\n\t"
      "global_load_dwordx4 v[52:55], v66, s[40:41] sc1\n\t"
      "s_waitcnt vmcnt(2)\n\t"
      "v_max3_u32 v56, v84, v85, v86\n\t"
      "v_max3_u32 v57, v87, v88, v89\n\t"
      "v_max3_u32 v56, v56, v90, v91\n\t"
      "v_pk_add_f32 v[84:85], v[84:85], v[88:89]\n\t"
      "v_pk_add_f32 v[86:87], v[86:87], v[90:91]\n\t"
      "s_waitcnt vmcnt(0)\n\t"
      "v_max3_u32 v56, v56, v48, v49\n\t"
      "v_max3_u32 v57, v57, v50, v51\n\t"
      "v_max3_u32 v56, v56, v52, v53\n\t"
      "v_max3_u32 v57, v57, v54, v55\n\t"
      "v_max_u32 v56, v56, v57\n\t"
      "v_pk_add_f32 v[48:49], v[48:49], v[52:53]\n\t"
      "v_cmp_eq_u32 vcc, -1, v56\n\t"
      "v_pk_add_f32 v[50:51], v[50:51], v[54:55]\n\t"
      "s_cbranch_vccz .Lwp3d\n\t"
      "s_sub_u32 s7, s7, 1\n\t"
      "s_cbranch_scc0 .Lwp3\n\t"
      ".Lwp3d:\n\t"
      "v_pk_add_f32 v[84:85], v[84:85], v[48:49]\n\t"
      "v_pk_add_f32 v[86:87], v[86:87], v[50:51]\n\t"
      "s_nop 1\n\t"
      "v_permlane32_swap_b32 v84, v86\n\t"
      "v_permlane32_swap_b32 v85, v87\n\t"
      "v_add_f32 v84, v84, v86\n\t"
      "v_add_f32 v85, v85, v87\n\t"
      "s_nop 1\n\t"
      "v_permlane16_swap_b32 v84, v85\n\t"
      "v_add_f32 v84, v84, v85\n\t"
      "s_nop 1\n\t"
      "v_add_f32_dpp v84, v84, v84 row_ror:4 row_mask:0xf bank_mask:0xf bound_ctrl:1\n\t"
      "s_nop 1\n\t"
      "v_add_f32_dpp v84, v84, v84 row_ror:8 row_mask:0xf bank_mask:0xf bound_ctrl:1\n\t"
      "v_cvt_pk_bf16_f32 v84, v84, v84\n\t"
      "v_lshlrev_b32 v84, 16, v84\n\t"
      "v_mul_f32 v84, v84, v97\n\t"
      "v_cvt_pk_bf16_f32 v84, v84, v84\n\t"
      "v_lshlrev_b32 v84, 16, v84\n\t"
      "v_add_f32 v84, v41, v84\n\t"
      "v_cvt_pk_bf16_f32 v84, v84, v84\n\t"
      "s_mov_b32 exec_lo, 0xf000f\n\t"
      "s_mov_b32 exec_hi, 0xf000f\n\t"
      "global_store_short v40, v84, s[56:57] nt\n\t"
      "s_cmp_lg_u32 s2, 0\n\t"
      "s_cbranch_scc1 .Lwend\n\t"
      "s_mov_b64 exec, 1\n\t"
      "v_mov_b32 v104, v27\n\t"
      "global_store_dwordx2 v14, v[104:105], s[48:49] sc1\n\t"
      "s_branch .Lwend\n\t"
      ".Lw1:\n\t"
      "s_load_dwordx2 s[54:55], s[0:1], 0x48\n\t"
      "s_waitcnt lgkmcnt(0)\n\t"
      "global_load_ushort v73, v14, s[54:55]\n\t"
      ".Lw1g:\n\t"
      "ds_read_b32 v46, v14 offset:28\n\t"
      "s_sleep 2\n\t"
      "s_waitcnt lgkmcnt(0)\n\t"
      "v_cmp_gt_u32 vcc, 4, v46\n\t"
      "s_cbranch_vccnz .Lw1g\n\t"
      "ds_read2_b32 v[46:47], v39 offset0:16 offset1:18\n\t"
      "ds_read2_b32 v[78:79], v39 offset0:20 offset1:22\n\t"
      "s_waitcnt lgkmcnt(0)\n\t"
      "v_pk_add_f32 v[46:47], v[46:47], v[78:79]\n\t"
      "v_add_f32 v46, v46, v47\n\t"
      "v_cvt_pk_bf16_f32 v46, v46, v46\n\t"
      "v_lshlrev_b32 v46, 16, v46\n\t"
      "s_waitcnt vmcnt(0)\n\t"
      "v_lshlrev_b32 v73, 16, v73\n\t"
      "v_add_f32 v46, v73, v46\n\t"
      "v_cvt_pk_bf16_f32 v46, v46, v46\n\t"
      "v_lshlrev_b32 v46, 16, v46\n\t"
      "v_mul_f32 v47, 0xbfb8aa3b, v46\n\t"
      "v_exp_f32 v47, v47\n\t"
      "s_nop 0\n\t"
      "v_add_f32 v47, 1.0, v47\n\t"
      "v_rcp_f32 v47, v47\n\t"
      "s_nop 0\n\t"
      "v_mul_f32 v46, v46, v47\n\t"
      "v_cvt_pk_bf16_f32 v46, v46, v46\n\t"
      "v_lshlrev_b32 v45, 16, v46\n\t"
      "s_mov_b32 exec_lo, 1\n\t"
      "s_mov_b32 exec_hi, 1\n\t"
      "global_store_dword v39, v45, s[26:27] offset:2560 sc1\n\t"
      "s_barrier\n\t"
      ".Lwb3:\n\t"
      "s_barrier\n\t"
      "s_branch .Lwend\n\t"
      ".Lw23:\n\t"
      "s_mov_b32 s7, 65536\n\t"
      "s_barrier\n\t"
      ".Lwp1:\n\t"
      "global_load_dwordx2 v[92:93], v30, s[24:25] sc1\n\t"
      "global_load_dwordx4 v[48:51], v31, s[16:17] sc1\n\t"
      "global_load_dwordx4 v[52:55], v31, s[18:19] sc1\n\t"
      "global_load_dwordx4 v[56:59], v28, s[16:17] sc1\n\t"
      "global_load_dwordx4 v[60:63], v28, s[18:19] sc1\n\t"
      "s_waitcnt vmcnt(4)\n\t"
      "v_max_f32_dpp v84, v92, v92 quad_perm:[1,0,3,2] row_mask:0xf bank_mask:0xf bound_ctrl:1\n\t"
      "v_max_u32 v86, v92, v93\n\t"
      "s_nop 0\n\t"
      "v_max_f32_dpp v84, v84, v84 quad_perm:[2,3,0,1] row_mask:0xf bank_mask:0xf bound_ctrl:1\n\t"
      "s_nop 1\n\t"
      "v_max_f32_dpp v84, v84, v84 row_ror:8 row_mask:0xf bank_mask:0xf bound_ctrl:1\n\t"
      "v_mov_b32 v85, v84\n\t"
      "s_nop 1\n\t"
      "v_permlane16_swap_b32 v84, v85\n\t"
      "v_max_f32 v84, v84, v85\n\t"
      "v_mov_b32 v85, v84\n\t"
      "s_nop 1\n\t"
      "v_permlane32_swap_b32 v84, v85\n\t"
      "v_max_f32 v84, v84, v85\n\t"
      "v_sub_f32 v84, v92, v84\n\t"
      "v_mul_f32 v84, v44, v84\n\t"
      "v_exp_f32 v84, v84\n\t"
      "s_waitcnt vmcnt(2)\n\t"
      "v_max3_u32 v86, v86, v48, v49\n\t"
      "v_mul_f32 v85, v84, v93\n\t"
      "v_mov_b32_dpp v94, v84 quad_perm:[0,0,0,0] row_mask:0xf bank_mask:0xf bound_ctrl:1\n\t"
      "v_mov_b32_dpp v98, v84 quad_perm:[1,1,1,1] row_mask:0xf bank_mask:0xf bound_ctrl:1\n\t"
      "v_add_f32_dpp v85, v85, v85 quad_perm:[1,0,3,2] row_mask:0xf bank_mask:0xf bound_ctrl:1\n\t"
      "v_mov_b32_dpp v100, v84 quad_perm:[2,2,2,2] row_mask:0xf bank_mask:0xf bound_ctrl:1\n\t"
      "v_mov_b32_dpp v102, v84 quad_perm:[3,3,3,3] row_mask:0xf bank_mask:0xf bound_ctrl:1\n\t"
      "v_add_f32_dpp v85, v85, v85 quad_perm:[2,3,0,1] row_mask:0xf bank_mask:0xf bound_ctrl:1\n\t"
      "v_max3_u32 v87, v50, v51, v52\n\t"
      "v_max3_u32 v86, v86, v53, v54\n\t"
      "v_add_f32_dpp v85, v85, v85 row_ror:8 row_mask:0xf bank_mask:0xf bound_ctrl:1\n\t"
      "v_mov_b32 v88, v85\n\t"
      "v_pk_mul_f32 v[48:49], v[48:49], v[94:95] op_sel_hi:[1,0]\n\t"
      "v_pk_mul_f32 v[50:51], v[50:51], v[94:95] op_sel_hi:[1,0]\n\t"
      "v_permlane16_swap_b32 v85, v88\n\t"
      "v_add_f32 v85, v85, v88\n\t"
      "v_mov_b32 v88, v85\n\t"
      "v_pk_fma_f32 v[48:49], v[52:53], v[98:99], v[48:49] op_sel_hi:[1,0,1]\n\t"
      "v_pk_fma_f32 v[50:51], v[54:55], v[98:99], v[50:51] op_sel_hi:[1,0,1]\n\t"
      "v_permlane32_swap_b32 v85, v88\n\t"
      "v_add_f32 v85, v85, v88\n\t"
      "v_rcp_f32 v85, v85\n\t"
      "s_waitcnt vmcnt(0)\n\t"
      "v_max3_u32 v87, v87, v55, v56\n\t"
      "v_max3_u32 v86, v86, v57, v58\n\t"
      "v_max3_u32 v87, v87, v59, v60\n\t"
      "v_max3_u32 v86, v86, v61, v62\n\t"
      "v_max3_u32 v86, v86, v87, v63\n\t"
      "v_pk_fma_f32 v[48:49], v[56:57], v[100:101], v[48:49] op_sel_hi:[1,0,1]\n\t"
      "v_cmp_eq_u32 vcc, -1, v86\n\t"
      "v_pk_fma_f32 v[50:51], v[58:59], v[100:101], v[50:51] op_sel_hi:[1,0,1]\n\t"
      "s_cbranch_vccz .Lwp1d\n\t"
      "s_sub_u32 s7, s7, 1\n\t"
      "s_cbranch_scc0 .Lwp1\n\t"
      ".Lwp1d:\n\t"
      "v_pk_fma_f32 v[48:49], v[60:61], v[102:103], v[48:49] op_sel_hi:[1,0,1]\n\t"
      "v_pk_fma_f32 v[50:51], v[62:63], v[102:103], v[50:51] op_sel_hi:[1,0,1]\n\t"
      "s_cmp_eq_u32 s4, 2\n\t"
      "s_cselect_b32 exec_lo, 0xff, 0\n\t"
      "s_mov_b32 exec_hi, 0\n\t"
      "global_atomic_add v69, v15, s[36:37] sc1\n\t"
      "s_mov_b64 exec, -1\n\t"
      "v_permlane32_swap_b32 v48, v50\n\t"
      "v_permlane32_swap_b32 v49, v51\n\t"
      "v_add_f32 v48, v48, v50\n\t"
      "v_add_f32 v49, v49, v51\n\t"
      "s_nop 1\n\t"
      "v_permlane16_swap_b32 v48, v49\n\t"
      "v_add_f32 v48, v48, v49\n\t"
      "s_nop 1\n\t"
      "v_add_f32_dpp v48, v48, v48 row_ror:8 row_mask:0xf bank_mask:0xf bound_ctrl:1\n\t"
      "v_mul_f32 v48, v48, v85\n\t"
      "v_cvt_pk_bf16_f32 v48, v48, v48\n\t"
      "ds_write_b16 v37, v48\n\t"
      "s_waitcnt lgkmcnt(0)\n\t"
      "s_barrier\n\t"
      ".Lwend:\n\t"
      "s_endpgm\n\t"
      ".Lkvmain:\n\t"
      "s_setprio 2\n\t"
      "v_add_u32 v7, 0x2000, v5\n\t"
      "v_xor_b32 v20, 32, v6\n\t"
      "s_add_u32 s10, s50, 0x1000\n\t"
      "s_addc_u32 s11, s51, 0\n\t"
      "v_mov_b32 v30, 0\n\t"
      "v_mov_b32 v31, 1\n\t"
      "v_and_b32 v21, 3, v1\n\t"
      "v_and_b32 v22, 0x30, v1\n\t"
      "v_bfe_u32 v23, v1, 2, 2\n\t"
      "v_lshlrev_b32 v24, 8, v21\n\t"
      "v_lshl_or_b32 v24, v22, 2, v24\n\t"
      "v_lshl_or_b32 v24, v23, 4, v24\n\t"
      "v_lshlrev_b32 v29, 4, v21\n\t"
      "s_lshl_b32 s7, s4, 2\n\t"
      "v_add_u32 v25, s7, v29\n\t"
      "v_lshl_or_b32 v100, v22, 2, v29\n\t"
      "s_add_u32 s7, s7, 2560\n\t"
      "v_add_u32 v26, s7, v100\n\t"
      "v_and_b32 v101, 1, v23\n\t"
      "v_lshlrev_b32 v101, 12, v101\n\t"
      "v_lshrrev_b32 v102, 1, v23\n\t"
      "v_lshl_or_b32 v101, v102, 10, v101\n\t"
      "v_lshl_or_b32 v101, v22, 4, v101\n\t"
      "v_lshrrev_b32 v102, 1, v21\n\t"
      "s_lshl_b32 s7, s4, 2\n\t"
      "v_or_b32 v102, s7, v102\n\t"
      "v_lshlrev_b32 v103, 1, v23\n\t"
      "v_xor_b32 v102, v102, v103\n\t"
      "v_lshrrev_b32 v103, 1, v22\n\t"
      "v_and_b32 v103, 8, v103\n\t"
      "v_xor_b32 v102, v102, v103\n\t"
      "v_lshl_or_b32 v101, v102, 4, v101\n\t"
      "v_and_b32 v102, 1, v21\n\t"
      "v_lshl_or_b32 v101, v102, 3, v101\n\t"
      "v_add_u32 v101, 24576, v101\n\t"
      "v_xor_b32 v102, 32, v101\n\t"
      "v_lshl_or_b32 v103, v21, 9, v22\n\t"
      "s_lshl_b32 s7, s4, 6\n\t"
      "s_add_u32 s7, s7, 3584\n\t"
      "v_lshlrev_b32 v27, 7, v22\n\t"
      "v_lshl_or_b32 v27, v23, 8, v27\n\t"
      "v_lshl_or_b32 v27, v21, 3, v27\n\t"
      "v_add_u32 v27, s7, v27\n\t"
      "s_lshl_b32 s7, s5, 5\n\t"
      "s_add_u32 s7, s7, s6\n\t"
      "s_mul_i32 s7, s7, 4224\n\t"
      "s_lshl_b32 s16, s4, 7\n\t"
      "s_add_u32 s7, s7, s16\n\t"
      "s_add_u32 s7, s7, 135168\n\t"
      "s_add_u32 s16, s48, s7\n\t"
      "s_addc_u32 s17, s49, 0\n\t"
      "v_lshlrev_b32 v28, 2, v1\n\t"
      "v_and_b32 v32, 16, v1\n\t"
      "v_cmp_ne_u32 s[18:19], 0, v32\n\t"
      "s_mov_b32 s22, 0x3db504f3\n\t"
      "s_mov_b32 s24, 0x3fb8aa3b\n\t"
      "s_lshl_b32 s7, s4, 10\n\t"
      "s_add_u32 m0, s7, 24576\n\t"
      "s_waitcnt vmcnt(3)\n\t"
      "s_add_u32 s12, s40, s14\n\t"
      "s_addc_u32 s13, s41, 0\n\t"
      "s_add_u32 s14, s14, 131072\n\t"
      "s_and_b32 s14, s14, 917504\n\t"
      "global_load_dwordx4 v[104:107], v3, s[12:13] nt\n\t"
      "s_waitcnt vmcnt(3)\n\t"
      "s_add_u32 s12, s40, s14\n\t"
      "s_addc_u32 s13, s41, 0\n\t"
      "s_add_u32 s14, s14, 131072\n\t"
      "s_and_b32 s14, s14, 917504\n\t"
      "global_load_dwordx4 v[108:111], v3, s[12:13] nt\n\t"
      "s_waitcnt vmcnt(3)\n\t"
      "s_add_u32 s12, s40, s14\n\t"
      "s_addc_u32 s13, s41, 0\n\t"
      "s_add_u32 s14, s14, 131072\n\t"
      "s_and_b32 s14, s14, 917504\n\t"
      "global_load_dwordx4 v[64:67], v3, s[12:13] nt\n\t"
      "s_waitcnt vmcnt(3)\n\t"
      "s_add_u32 s12, s40, s14\n\t"
      "s_addc_u32 s13, s41, 0\n\t"
      "s_add_u32 s14, s14, 131072\n\t"
      "s_and_b32 s14, s14, 917504\n\t"
      "global_load_dwordx4 v[68:71], v3, s[12:13] nt\n\t"
      "s_waitcnt vmcnt(3)\n\t"
      "s_add_u32 s12, s50, s14\n\t"
      "s_addc_u32 s13, s51, 0\n\t"
      "s_add_u32 s14, s14, 131072\n\t"
      "s_and_b32 s14, s14, 917504\n\t"
      "global_load_lds_dwordx4 v6, s[12:13] nt\n\t"
      "s_add_u32 m0, m0, 0x1000\n\t"
      "s_waitcnt vmcnt(3)\n\t"
      "s_add_u32 s12, s50, s14\n\t"
      "s_addc_u32 s13, s51, 0\n\t"
      "s_add_u32 s14, s14, 131072\n\t"
      "s_and_b32 s14, s14, 917504\n\t"
      "global_load_lds_dwordx4 v20, s[12:13] nt\n\t"
      "s_add_u32 m0, m0, 0x1000\n\t"
      "s_waitcnt vmcnt(3)\n\t"
      "s_add_u32 s12, s50, s14\n\t"
      "s_addc_u32 s13, s51, 0\n\t"
      "s_add_u32 s14, s14, 131072\n\t"
      "s_and_b32 s14, s14, 917504\n\t"
      "global_load_lds_dwordx4 v6, s[12:13] nt\n\t"
      "s_add_u32 m0, m0, 0x1000\n\t"
      "s_waitcnt vmcnt(3)\n\t"
      "s_add_u32 s12, s50, s14\n\t"
      "s_addc_u32 s13, s51, 0\n\t"
      "s_add_u32 s14, s14, 131072\n\t"
      "s_and_b32 s14, s14, 917504\n\t"
      "global_load_lds_dwordx4 v20, s[12:13] nt\n\t"
      "s_add_u32 m0, m0, 0x1000\n\t"
      "s_mov_b64 exec, 1\n\t"
      "ds_add_u32 v30, v31 offset:16\n\t"
      "s_mov_b64 exec, -1\n\t"
      ".Lkhsa:\n\t"
      "ds_read_b32 v76, v30 offset:24\n\t"
      "s_waitcnt lgkmcnt(0)\n\t"
      "v_cmp_gt_u32 vcc, 4, v76\n\t"
      "s_cbranch_vccnz .Lkhsa\n\t"
      "s_add_u32 s12, s50, s14\n\t"
      "s_addc_u32 s13, s51, 0\n\t"
      "s_add_u32 s14, s14, 131072\n\t"
      "s_and_b32 s14, s14, 917504\n\t"
      "global_load_lds_dwordx4 v6, s[12:13] nt\n\t"
      "s_add_u32 m0, m0, 0x1000\n\t"
      "s_add_u32 s12, s50, s14\n\t"
      "s_addc_u32 s13, s51, 0\n\t"
      "s_add_u32 s14, s14, 131072\n\t"
      "s_and_b32 s14, s14, 917504\n\t"
      "global_load_lds_dwordx4 v20, s[12:13] nt\n\t"
      "s_add_u32 m0, m0, 0x1000\n\t"
      "s_add_u32 s12, s50, s14\n\t"
      "s_addc_u32 s13, s51, 0\n\t"
      "s_add_u32 s14, s14, 131072\n\t"
      "s_and_b32 s14, s14, 917504\n\t"
      "global_load_lds_dwordx4 v6, s[12:13] nt\n\t"
      "s_add_u32 m0, m0, 0x1000\n\t"
      "s_add_u32 s12, s50, s14\n\t"
      "s_addc_u32 s13, s51, 0\n\t"
      "s_add_u32 s14, s14, 131072\n\t"
      "s_and_b32 s14, s14, 917504\n\t"
      "global_load_lds_dwordx4 v20, s[12:13] nt\n\t"
      "s_waitcnt vmcnt(8)\n\t"
      ".Lkb1:\n\t"
      "ds_read_b32 v76, v30 offset:8\n\t"
      "ds_read_b128 v[72:75], v24 offset:1536\n\t"
      "s_waitcnt lgkmcnt(0)\n\t"
      "v_cmp_gt_u32 vcc, 4, v76\n\t"
      "s_cbranch_vccnz .Lkb1\n\t"
      "v_mfma_f32_16x16x32_bf16 v[112:115], v[112:115], v[72:75], 0\n\t"
      "s_nop 1\n\t"
      "v_mfma_f32_16x16x32_bf16 v[116:119], v[116:119], v[72:75], 0\n\t"
      "s_nop 1\n\t"
      "v_mfma_f32_16x16x32_bf16 v[120:123], v[120:123], v[72:75], 0\n\t"
      "s_nop 1\n\t"
      "v_mfma_f32_16x16x32_bf16 v[124:127], v[124:127], v[72:75], 0\n\t"
      "v_add_f32_dpp v8, v113, v112 row_shl:4 row_mask:0xf bank_mask:0xf bound_ctrl:1\n\t"
      "s_nop 0\n\t"
      "v_mfma_f32_16x16x32_bf16 v[104:107], v[104:107], v[72:75], 0\n\t"
      "v_add_f32_dpp v9, v117, v116 row_shl:4 row_mask:0xf bank_mask:0xf bound_ctrl:1\n\t"
      "v_add_f32_dpp v8, v114, v8 row_shl:8 row_mask:0xf bank_mask:0xf bound_ctrl:1\n\t"
      "v_mfma_f32_16x16x32_bf16 v[108:111], v[108:111], v[72:75], 0\n\t"
      "v_add_f32_dpp v10, v121, v120 row_shl:4 row_mask:0xf bank_mask:0xf bound_ctrl:1\n\t"
      "v_add_f32_dpp v9, v118, v9 row_shl:8 row_mask:0xf bank_mask:0xf bound_ctrl:1\n\t"
      "v_mfma_f32_16x16x32_bf16 v[64:67], v[64:67], v[72:75], 0\n\t"
      "v_add_f32_dpp v8, v115, v8 row_shl:12 row_mask:0xf bank_mask:0xf bound_ctrl:1\n\t"
      "v_add_f32_dpp v11, v125, v124 row_shl:4 row_mask:0xf bank_mask:0xf bound_ctrl:1\n\t"
      "v_mfma_f32_16x16x32_bf16 v[68:71], v[68:71], v[72:75], 0\n\t"
      "v_add_f32_dpp v10, v122, v10 row_shl:8 row_mask:0xf bank_mask:0xf bound_ctrl:1\n\t"
      "v_add_f32_dpp v9, v119, v9 row_shl:12 row_mask:0xf bank_mask:0xf bound_ctrl:1\n\t"
      "v_add_f32_dpp v12, v105, v104 row_shl:4 row_mask:0xf bank_mask:0xf bound_ctrl:1\n\t"
      "v_add_f32_dpp v11, v126, v11 row_shl:8 row_mask:0xf bank_mask:0xf bound_ctrl:1\n\t"
      "v_add_f32_dpp v10, v123, v10 row_shl:12 row_mask:0xf bank_mask:0xf bound_ctrl:1\n\t"
      "v_add_f32_dpp v13, v109, v108 row_shl:4 row_mask:0xf bank_mask:0xf bound_ctrl:1\n\t"
      "v_add_f32_dpp v12, v106, v12 row_shl:8 row_mask:0xf bank_mask:0xf bound_ctrl:1\n\t"
      "v_add_f32_dpp v11, v127, v11 row_shl:12 row_mask:0xf bank_mask:0xf bound_ctrl:1\n\t"
      "v_add_f32_dpp v14, v65, v64 row_shl:4 row_mask:0xf bank_mask:0xf bound_ctrl:1\n\t"
      "v_add_f32_dpp v13, v110, v13 row_shl:8 row_mask:0xf bank_mask:0xf bound_ctrl:1\n\t"
      "v_add_f32_dpp v12, v107, v12 row_shl:12 row_mask:0xf bank_mask:0xf bound_ctrl:1\n\t"
      "v_add_f32_dpp v15, v69, v68 row_shl:4 row_mask:0xf bank_mask:0xf bound_ctrl:1\n\t"
      "v_add_f32_dpp v14, v66, v14 row_shl:8 row_mask:0xf bank_mask:0xf bound_ctrl:1\n\t"
      "v_add_f32_dpp v13, v111, v13 row_shl:12 row_mask:0xf bank_mask:0xf bound_ctrl:1\n\t"
      "v_add_f32_dpp v15, v70, v15 row_shl:8 row_mask:0xf bank_mask:0xf bound_ctrl:1\n\t"
      "v_add_f32_dpp v14, v67, v14 row_shl:12 row_mask:0xf bank_mask:0xf bound_ctrl:1\n\t"
      "s_nop 0\n\t"
      "v_add_f32_dpp v15, v71, v15 row_shl:12 row_mask:0xf bank_mask:0xf bound_ctrl:1\n\t"
      "v_max3_f32 v16, v8, v9, v10\n\t"
      "v_max3_f32 v17, v11, v12, v13\n\t"
      "v_max3_f32 v16, v16, v14, v15\n\t"
      "v_max_f32 v16, v16, v17\n\t"
      "v_mov_b32 v17, v16\n\t"
      "v_cvt_pk_bf16_f32 v9, v8, v9\n\t"
      "v_cvt_pk_bf16_f32 v11, v10, v11\n\t"
      "v_permlane16_swap_b32 v16, v17\n\t"
      "v_max_f32 v16, v16, v17\n\t"
      "v_mov_b32 v17, v16\n\t"
      "v_cvt_pk_bf16_f32 v13, v12, v13\n\t"
      "v_cvt_pk_bf16_f32 v15, v14, v15\n\t"
      "v_permlane32_swap_b32 v16, v17\n\t"
      "v_max_f32 v16, v16, v17\n\t"
      "v_cvt_pk_bf16_f32 v16, v16, v16\n\t"
      "v_lshlrev_b32 v16, 16, v16\n\t"
      "v_mul_f32 v16, s22, v16\n\t"
      "v_cvt_pk_bf16_f32 v16, v16, v16\n\t"
      "v_lshlrev_b32 v76, 16, v16\n\t"
      "s_mov_b64 exec, 15\n\t"
      "ds_write_b32 v25, v76 offset:128\n\t"
      "s_mov_b64 exec, 1\n\t"
      "ds_add_u32 v30, v31 offset:4\n\t"
      "s_mov_b64 exec, -1\n\t"
      "v_lshlrev_b32 v8, 16, v9\n\t"
      "v_and_b32 v9, 0xffff0000, v9\n\t"
      "v_pk_mul_f32 v[8:9], v[8:9], s[22:23] op_sel_hi:[1,0]\n\t"
      "v_cvt_pk_bf16_f32 v9, v8, v9\n\t"
      "v_lshlrev_b32 v8, 16, v9\n\t"
      "v_and_b32 v9, 0xffff0000, v9\n\t"
      "v_lshlrev_b32 v10, 16, v11\n\t"
      "v_and_b32 v11, 0xffff0000, v11\n\t"
      "v_pk_mul_f32 v[10:11], v[10:11], s[22:23] op_sel_hi:[1,0]\n\t"
      "v_cvt_pk_bf16_f32 v11, v10, v11\n\t"
      "ds_read_b32 v76, v30 offset:4\n\t"
      "ds_read_b128 v[16:19], v29 offset:128\n\t"
      "v_lshlrev_b32 v10, 16, v11\n\t"
      "v_and_b32 v11, 0xffff0000, v11\n\t"
      "v_lshlrev_b32 v12, 16, v13\n\t"
      "v_and_b32 v13, 0xffff0000, v13\n\t"
      "v_pk_mul_f32 v[12:13], v[12:13], s[22:23] op_sel_hi:[1,0]\n\t"
      "v_cvt_pk_bf16_f32 v13, v12, v13\n\t"
      "v_lshlrev_b32 v12, 16, v13\n\t"
      "v_and_b32 v13, 0xffff0000, v13\n\t"
      "v_lshlrev_b32 v14, 16, v15\n\t"
      "v_and_b32 v15, 0xffff0000, v15\n\t"
      "v_pk_mul_f32 v[14:15], v[14:15], s[22:23] op_sel_hi:[1,0]\n\t"
      "v_cvt_pk_bf16_f32 v15, v14, v15\n\t"
      "v_lshlrev_b32 v14, 16, v15\n\t"
      "v_and_b32 v15, 0xffff0000, v15\n\t"
      ".Lkm1:\n\t"
      "s_waitcnt lgkmcnt(0)\n\t"
      "v_cmp_gt_u32 vcc, 4, v76\n\t"
      "v_max3_f32 v16, v16, v17, v18\n\t"
      "s_cbranch_vccnz .Lkm\n\t"
      "v_max_f32 v16, v16, v19\n\t"
      "v_mul_f32 v78, s24, v16\n\t"
      "v_pk_fma_f32 v[8:9], v[8:9], s[24:25], v[78:79] op_sel_hi:[1,0,0] neg_lo:[0,0,1] neg_hi:[0,0,1]\n\t"
      "v_pk_fma_f32 v[10:11], v[10:11], s[24:25], v[78:79] op_sel_hi:[1,0,0] neg_lo:[0,0,1] neg_hi:[0,0,1]\n\t"
      "v_pk_fma_f32 v[12:13], v[12:13], s[24:25], v[78:79] op_sel_hi:[1,0,0] neg_lo:[0,0,1] neg_hi:[0,0,1]\n\t"
      "v_pk_fma_f32 v[14:15], v[14:15], s[24:25], v[78:79] op_sel_hi:[1,0,0] neg_lo:[0,0,1] neg_hi:[0,0,1]\n\t"
      "v_exp_f32 v8, v8\n\t"
      "v_exp_f32 v9, v9\n\t"
      "v_exp_f32 v10, v10\n\t"
      "v_exp_f32 v11, v11\n\t"
      "v_exp_f32 v12, v12\n\t"
      "v_exp_f32 v13, v13\n\t"
      "v_exp_f32 v14, v14\n\t"
      "v_exp_f32 v15, v15\n\t"
      "s_mov_b32 exec_lo, 0xf000f\n\t"
      "s_mov_b32 exec_hi, 0xf000f\n\t"
      "v_cvt_pk_bf16_f32 v16, v8, v9\n\t"
      "v_cvt_pk_bf16_f32 v17, v10, v11\n\t"
      "v_cvt_pk_bf16_f32 v18, v12, v13\n\t"
      "v_cvt_pk_bf16_f32 v19, v14, v15\n\t"
      "ds_write2_b32 v26, v16, v17 offset1:64\n\t"
      "ds_write2_b32 v26, v18, v19 offset0:128 offset1:192\n\t"
      "s_mov_b64 exec, 1\n\t"
      "ds_add_u32 v30, v31 offset:32\n\t"
      "s_mov_b32 exec_lo, 0xf000f\n\t"
      "s_mov_b32 exec_hi, 0xf000f\n\t"
      "v_pk_add_f32 v[78:79], v[8:9], v[10:11]\n\t"
      "v_pk_add_f32 v[80:81], v[12:13], v[14:15]\n\t"
      "v_pk_add_f32 v[78:79], v[78:79], v[80:81]\n\t"
      "v_add_f32 v77, v78, v79\n\t"
      "v_mov_b32 v78, v77\n\t"
      "s_nop 1\n\t"
      "v_permlane16_swap_b32 v77, v78\n\t"
      "v_add_f32 v77, v77, v78\n\t"
      "v_mov_b32 v78, v77\n\t"
      "s_nop 1\n\t"
      "v_permlane32_swap_b32 v77, v78\n\t"
      "v_add_f32 v77, v77, v78\n\t"
      "ds_write_b32 v25, v77 offset:192\n\t"
      "s_mov_b64 exec, 1\n\t"
      "s_waitcnt vmcnt(4)\n\t"
      "ds_add_u32 v30, v31 offset:36\n\t"
      "s_mov_b64 exec, -1\n\t"
      ".Lkp:\n\t"
      "ds_read_b128 v[76:79], v30 offset:32\n\t"
      "ds_read_b128 v[32:35], v100 offset:2560\n\t"
      "ds_read_b128 v[36:39], v100 offset:2816\n\t"
      "ds_read_b128 v[40:43], v100 offset:3072\n\t"
      "ds_read_b128 v[44:47], v100 offset:3328\n\t"
      "ds_read_b64_tr_b16 v[48:49], v101 offset:0\n\t"
      "ds_read_b64_tr_b16 v[50:51], v101 offset:2048\n\t"
      "ds_read_b64_tr_b16 v[52:53], v102 offset:0\n\t"
      "ds_read_b64_tr_b16 v[54:55], v102 offset:2048\n\t"
      "ds_read_b64_tr_b16 v[56:57], v101 offset:8192\n\t"
      "ds_read_b64_tr_b16 v[58:59], v101 offset:10240\n\t"
      "ds_read_b64_tr_b16 v[60:61], v102 offset:8192\n\t"
      "ds_read_b64_tr_b16 v[62:63], v102 offset:10240\n\t"
      "s_waitcnt lgkmcnt(0)\n\t"
      "v_min3_u32 v76, v76, v77, v78\n\t"
      "v_cmp_gt_u32 vcc, 4, v76\n\t"
      "s_cbranch_vccnz .Lkp\n\t"
      "v_mfma_f32_16x16x32_bf16 v[128:131], v[48:51], v[32:35], 0\n\t"
      "v_mfma_f32_16x16x32_bf16 v[132:135], v[52:55], v[32:35], 0\n\t"
      "v_mfma_f32_16x16x32_bf16 v[128:131], v[56:59], v[36:39], v[128:131]\n\t"
      "v_mfma_f32_16x16x32_bf16 v[132:135], v[60:63], v[36:39], v[132:135]\n\t"
      "s_mov_b64 exec, 1\n\t"
      "s_waitcnt vmcnt(0)\n\t"
      "ds_add_u32 v30, v31 offset:12\n\t"
      "s_mov_b64 exec, -1\n\t"
      ".Lkv:\n\t"
      "ds_read_b32 v76, v30 offset:12\n\t"
      "ds_read_b64_tr_b16 v[84:85], v101 offset:16384\n\t"
      "ds_read_b64_tr_b16 v[86:87], v101 offset:18432\n\t"
      "ds_read_b64_tr_b16 v[88:89], v102 offset:16384\n\t"
      "ds_read_b64_tr_b16 v[90:91], v102 offset:18432\n\t"
      "ds_read_b64_tr_b16 v[92:93], v101 offset:24576\n\t"
      "ds_read_b64_tr_b16 v[94:95], v101 offset:26624\n\t"
      "ds_read_b64_tr_b16 v[96:97], v102 offset:24576\n\t"
      "ds_read_b64_tr_b16 v[98:99], v102 offset:26624\n\t"
      "s_waitcnt lgkmcnt(0)\n\t"
      "v_cmp_gt_u32 vcc, 4, v76\n\t"
      "s_cbranch_vccnz .Lkv\n\t"
      "v_mfma_f32_16x16x32_bf16 v[128:131], v[84:87], v[40:43], v[128:131]\n\t"
      "v_mfma_f32_16x16x32_bf16 v[132:135], v[88:91], v[40:43], v[132:135]\n\t"
      "v_mfma_f32_16x16x32_bf16 v[128:131], v[92:95], v[44:47], v[128:131]\n\t"
      "v_mfma_f32_16x16x32_bf16 v[132:135], v[96:99], v[44:47], v[132:135]\n\t"
      "s_mov_b32 exec_lo, 0xf000f\n\t"
      "s_mov_b32 exec_hi, 0xf000f\n\t"
      "s_nop 4\n\t"
      "global_store_dwordx4 v103, v[128:131], s[16:17] sc1\n\t"
      "global_store_dwordx4 v103, v[132:135], s[16:17] offset:64 sc1\n\t"
      "s_mov_b64 exec, -1\n\t"
      "s_barrier\n\t"
      "ds_read_b64_tr_b16 v[32:33], v27\n\t"
      "ds_read_b64_tr_b16 v[34:35], v27 offset:1024\n\t"
      "ds_read_b64_tr_b16 v[36:37], v27 offset:8192\n\t"
      "ds_read_b64_tr_b16 v[38:39], v27 offset:9216\n\t"
      "ds_read_b64_tr_b16 v[40:41], v27 offset:32\n\t"
      "ds_read_b64_tr_b16 v[42:43], v27 offset:1056\n\t"
      "ds_read_b64_tr_b16 v[44:45], v27 offset:8224\n\t"
      "ds_read_b64_tr_b16 v[46:47], v27 offset:9248\n\t"
      "s_waitcnt lgkmcnt(0)\n\t"
      "s_barrier\n\t"
      "ds_read_b128 v[48:51], v22 offset:256\n\t"
      "ds_read_b128 v[52:55], v22 offset:320\n\t"
      "s_mov_b32 exec_hi, 0\n\t"
      "s_waitcnt lgkmcnt(1)\n\t"
      "v_mfma_f32_16x16x32_bf16 v[56:59], v[48:51], v[32:35], 0\n\t"
      "v_mfma_f32_16x16x32_bf16 v[60:63], v[48:51], v[40:43], 0\n\t"
      "s_waitcnt lgkmcnt(0)\n\t"
      "v_mfma_f32_16x16x32_bf16 v[56:59], v[52:55], v[36:39], v[56:59]\n\t"
      "v_mfma_f32_16x16x32_bf16 v[60:63], v[52:55], v[44:47], v[60:63]\n\t"
      "s_nop 7\n\t"
      "v_cndmask_b32 v56, v56, v60, s[18:19]\n\t"
      "global_store_dword v28, v56, s[16:17] offset:2048 sc1\n\t"
      "s_endpgm\n\t"
      ".Lkm:\n\t"
      "ds_read_b32 v76, v30 offset:4\n\t"
      "ds_read_b128 v[16:19], v29 offset:128\n\t"
      "s_branch .Lkm1\n\t"
      ".Lgmain:\n\t"
      "v_mov_b32 v15, 1\n\t"
      "s_mov_b64 exec, 1\n\t"
      "s_waitcnt vmcnt(0) lgkmcnt(0)\n\t"
      "ds_add_rtn_u32 v5, v14, v15 offset:40\n\t"
      "v_add_u32 v104, 1, v104\n\t"
      "s_waitcnt lgkmcnt(0)\n\t"
      "v_cmp_eq_u32 vcc, 3, v5\n\t"
      "s_cbranch_vccz .Lgnot\n\t"
      "global_store_dword v14, v104, s[28:29] offset:3072 sc1\n\t"
      ".Lgnot:\n\t"
      "s_mov_b64 exec, -1\n\t"
      : "+{s[0:1]}"(ap), "+{s2}"(b), "+{v0}"(tid)
      : "s"((unsigned)(size_t)L)
      : "memory", "scc", "vcc", "s3", "s4", "s5", "s6", "s7", "s8", "s9", S10(1), S10(2), "s30", "s31", "s34", "s35", "s36", "s37", "s38", "s39", S10(4), S10(5), "s60", "s61", "s62", "s63", "s64", "s65",
        "v1", "v2", "v3", "v4", "v5", "v6", "v7", "v8", "v9", V10(1), V10(2), V10(3), V10(4), V10(5), V10(6), V10(7), V10(8), V10(9), V10(10), V10(11), V10(12), V10(13));
  const int g = b & 7, j = b >> 3, lane = tid & 63, w = __builtin_amdgcn_readfirstlane(tid >> 6) & 3;
  const int c0 = 64 * g + 2 * j;
  v4u xv, p0, p1, p2;
  asm volatile("" : "={v[108:111]}"(xv), "={v[112:115]}"(p0), "={v[116:119]}"(p1), "={v[120:123]}"(p2) :: "memory");
  if (w * 64 + lane >= 224) p2 = v4u{0u, 0u, 0u, 0u};
  float xf[8] = {bflo(xv.x), bfhi(xv.x), bflo(xv.y), bfhi(xv.y), bflo(xv.z), bfhi(xv.z), bflo(xv.w), bfhi(xv.w)};
  const unsigned xa = __builtin_amdgcn_readlane(xv.x, c0 >> 3), xb = __builtin_amdgcn_readlane(xv.y, c0 >> 3), xc = __builtin_amdgcn_readlane(xv.z, c0 >> 3), xd = __builtin_amdgcn_readlane(xv.w, c0 >> 3);
  const unsigned xp = (j & 2) ? ((j & 1) ? xd : xc) : ((j & 1) ? xb : xa);
  const LN s1 = ln_stats(xf);
#pragma unroll
  for (int i = 0; i < 8; i += 2) ln_apply2(xf[i], xf[i + 1], s1);
  const LN s2 = ln_stats(xf);
  float h0 = bflo(xp), h1 = bfhi(xp);
  ln_apply2(h0, h1, s1); ln_apply2(h0, h1, s2);
  constexpr float K0 = -2.0f * 0.7978845608028654f * 1.4426950408889634f, K1 = K0 * 0.044715f;
  float a0 = 0.f, a1 = 0.f;
#pragma unroll
  for (int e = 0; e < 8; ++e) {
    const float wv = (e & 1) ? bfhi(p0[e >> 1]) : bflo(p0[e >> 1]), bv = (e & 1) ? bfhi(p1[e >> 1]) : bflo(p1[e >> 1]), vv = (e & 1) ? bfhi(p2[e >> 1]) : bflo(p2[e >> 1]);
    float u0 = h0 * wv, u1 = h1 * wv; rbf2(u0, u1);
    u0 += bv; u1 += bv; rbf2(u0, u1);
    float y0 = u0 * __builtin_amdgcn_rcpf(1.0f + __builtin_amdgcn_exp2f(u0 * fmaf(u0 * u0, K1, K0)));
    float y1 = u1 * __builtin_amdgcn_rcpf(1.0f + __builtin_amdgcn_exp2f(u1 * fmaf(u1 * u1, K1, K0)));
    rbf2(y0, y1);
    a0 = fmaf(y0, vv, a0); a1 = fmaf(y1, vv, a1);
  }
  float t = sw32add(a0, a1); t = rowsum(t); t = sw16add(t, t);
  if ((lane & 31) == 0) ((LAS float*)(L + L_GATE))[w * 2 + (lane >> 5)] = t;
  {
    typedef __attribute__((address_space(4))) const Args* CArgs;
    const char* wo = ((CArgs)ap)->wo;
    const unsigned off = (unsigned)(g * 512 + (j >> 2) * 64 + 4 * w + (lane >> 4)) * 1024u + (j & 3) * 256 + (lane & 15) * 16;
    unsigned t0;
    asm volatile("s_mov_b64 exec, 1\n\tds_add_u32 %1, %8 offset:28\n\ts_mov_b64 exec, -1\n.Lgw:\n\tds_read_b32 %0, %1 offset:8\n\ts_sleep 1\n\ts_waitcnt lgkmcnt(0)\n\tv_cmp_gt_u32 vcc, 4, %0\n\ts_cbranch_vccnz .Lgw\n\t"
                 "s_mov_b32 m0, %5\n\ts_nop 0\n\tglobal_load_lds_dwordx4 %2, %3 nt\n\ts_add_u32 m0, m0, 0x1000\n\ts_nop 0\n\tglobal_load_lds_dwordx4 %4, %3 nt\n\t"
                 "s_add_u32 m0, m0, 0x1000\n\ts_nop 0\n\tglobal_load_lds_dwordx4 %6, %3 nt\n\ts_add_u32 m0, m0, 0x1000\n\ts_nop 0\n\tglobal_load_lds_dwordx4 %7, %3 nt\n\t"
                 "s_waitcnt vmcnt(0)"
                 : "=&v"(t0) : "v"(0u), "v"(off), "s"(wo), "v"(off + 16384u), "s"(L_WO + w * 1024), "v"(off + 32768u), "v"(off + 49152u), "v"(1u) : "memory", "vcc", "scc");
  }
}
}

extern "C" void kernel_launch(void* const* d_in, const int* in_sizes, int n_in, void* d_out, int out_size, void* d_ws, size_t ws_size, hipStream_t stream) {
  fused::Args a;
  a.x = (const char*)d_in[0]; a.K = (const char*)d_in[1]; a.V = (const char*)d_in[2]; a.wq = (const char*)d_in[3]; a.wo = (const char*)d_in[4];
  a.w1 = (const char*)d_in[6]; a.b1 = (const char*)d_in[7]; a.w2 = (const char*)d_in[8]; a.b2 = (const char*)d_in[9];
  a.out = (char*)d_out; a.ws = (char*)d_ws;
  fused::fused_kernel<<<256, 768, 0, stream>>>(a);
}
```
